```python
import torch

def _softmax(x, axis=-1):
    return torch.softmax(x, dim=axis)
def _gelu(x):
    return torch.nn.functional.gelu(x, approximate="tanh")
def _silu(x):
    return torch.nn.functional.silu(x)
def _sigmoid(x):
    return torch.sigmoid(x)
def _layernorm(x, axis=-1, eps=1e-5):
    m = x.mean(dim=axis, keepdim=True)
    v = x.var(dim=axis, keepdim=True, unbiased=False)
    return (x - m) / torch.sqrt(v + eps)
def _scatter_add(tgt, idx, src, axis=0):
    return tgt.clone().index_add_(axis, idx, src)
def _segment_sum(src, seg, n):
    out = torch.zeros((n,) + src.shape[1:], dtype=src.dtype)
    return out.index_add_(0, seg, src)
def _pad(x, pw):
    flat = tuple(v for pair in reversed(pw) for v in pair)
    return torch.nn.functional.pad(x, flat)

kernel_name = 'standalone__self_sup_conv_stem_hubert__speech'

def setup_inputs():

    _w = (lambda a: (lambda t: t.to(torch.bfloat16) if t.is_floating_point() else t)(torch.as_tensor(a)))
    B, T, D, K = 2, 4000, 512, 15
    scale = (1.0 / (K * D)) ** 0.5
    return {
        "x": _w((lambda *s: torch.randn(*s, dtype=torch.bfloat16) * 0.05)(B, T, D)),
        "w": _w((lambda *s: torch.randn(*s, dtype=torch.bfloat16) * 0.05)(K, D, D) * scale),
        "b": _w((lambda *s: torch.randn(*s, dtype=torch.bfloat16) * 0.05)(D) * 0.1),
        "gamma": _w(torch.ones((D,), dtype=float)),
        "beta": _w(torch.zeros((D,), dtype=float)),
    }

def reference(x, w, b, gamma, beta):
    B, T, D = x.shape
    K = w.shape[0]
    s = 2
    L = (T - K) // s + 1
    y = torch.einsum('blc,cd->bld', x[:, 0 : s * L : s, :], w[0])
    for i in range(1, K):
        y = y + torch.einsum('blc,cd->bld', x[:, i : i + s * L : s, :], w[i])
    y = y + b[None, None, :]
    mu = y.mean(1, keepdims=True)
    var = ((y - mu) ** 2).mean(1, keepdims=True)
    yn = (y - mu) / torch.sqrt(var + 1e-5)
    yn = yn * gamma[None, None, :] + beta[None, None, :]
    return _gelu(yn)
```

```cpp
#include <hip/hip_runtime.h>
#include <hip/hip_bf16.h>

typedef __bf16 bf16x4 __attribute__((ext_vector_type(4)));
typedef __bf16 bf16x8 __attribute__((ext_vector_type(8)));
typedef __bf16 bf16x2 __attribute__((ext_vector_type(2)));
typedef float f32x4 __attribute__((ext_vector_type(4)));
typedef float f32x2 __attribute__((ext_vector_type(2)));
typedef unsigned u32x2 __attribute__((ext_vector_type(2)));
typedef unsigned u32x4 __attribute__((ext_vector_type(4)));
#define LDS_AS __attribute__((address_space(3)))

constexpr int NB = 2, T = 4000, D = 512, KT = 15, L = 1993;
constexpr int BM = 256, BN = 32;
constexpr int XIMG = 272 * 128;
constexpr int WIMG = 8 * 64 * 64;
constexpr int STG = XIMG + WIMG;
constexpr int LDS_BYTES = 2 * STG;

__device__ __forceinline__ void dma16(__amdgpu_buffer_rsrc_t rs, unsigned ldsaddr, unsigned voff, unsigned soff) {
  asm volatile("s_mov_b32 m0, %0\n\ts_nop 0\n\tbuffer_load_dwordx4 %1, %2, %3 offen lds"
               :: "s"(ldsaddr), "v"(voff), "s"(rs), "s"(soff) : "memory");
}

__device__ __forceinline__ float row16_sum(float v) {
  v += __builtin_bit_cast(float, __builtin_amdgcn_update_dpp(0, __builtin_bit_cast(int, v), 0xB1, 0xF, 0xF, true));
  v += __builtin_bit_cast(float, __builtin_amdgcn_update_dpp(0, __builtin_bit_cast(int, v), 0x4E, 0xF, 0xF, true));
  v += __builtin_bit_cast(float, __builtin_amdgcn_update_dpp(0, __builtin_bit_cast(int, v), 0x141, 0xF, 0xF, true));
  v += __builtin_bit_cast(float, __builtin_amdgcn_update_dpp(0, __builtin_bit_cast(int, v), 0x140, 0xF, 0xF, true));
  return v;
}

__global__ __launch_bounds__(512, 2)
void conv_gemm_kernel(const __bf16* __restrict__ x, const __bf16* __restrict__ w, const __bf16* __restrict__ bias,
                      float* __restrict__ y, f32x2* __restrict__ part) {
  __shared__ __attribute__((aligned(1024))) char smem[LDS_BYTES];
  const int tid = threadIdx.x, lane = tid & 63;
  const int wv = __builtin_amdgcn_readfirstlane(tid >> 6);
  const int bid = blockIdx.x;
  const int xcd = bid & 7, idx = bid >> 3;
  const int mt = (xcd & 1) * 8 + (idx & 7), nt = (xcd >> 1) * 4 + (idx >> 3);
  const int b = mt >> 3, l0 = (mt & 7) * BM, n0 = nt * BN;
  const int wm = wv & 1, nh = (wv >> 1) & 1, kc = wv >> 2;

  const __amdgpu_buffer_rsrc_t rx = __builtin_amdgcn_make_buffer_rsrc((void*)x, 0, NB * T * D * 2, 0x00020000);
  const __amdgpu_buffer_rsrc_t rw = __builtin_amdgcn_make_buffer_rsrc((void*)w, 0, KT * D * D * 2, 0x00020000);
  const unsigned sbase = (unsigned)(size_t)(LDS_AS char*)smem;

  unsigned xvoff;
  {
    const int u = lane >> 3, pos = lane & 7;
    const int trow = 16 * (wv >> 1) + 4 * (wv & 1) + (u >> 1) + 8 * (u & 1);
    const int ch = pos ^ (((wv >> 1) & 3) << 1);
    xvoff = (unsigned)((b * T + 2 * l0 + 2 * trow) * (D * 2) + ch * 16);
  }
  unsigned wvoff;
  {
    const int row = lane >> 2, pos = lane & 3;
    const int ch = pos ^ (((row >> 3) & 1) << 1);
    wvoff = (unsigned)(((wv >> 2) * 2 * D + 16 * (wv & 3) + row) * (D * 2) + n0 * 2 + ch * 16);
  }
  auto issue_piece = [&](int s, int k) {
    const int p = s & 1, cc = s >> 1;
    const unsigned lb = sbase + (unsigned)((s & 1) * STG);
    if (k < 4) {
      dma16(rx, lb + (unsigned)((wv + 8 * k) * 1024), xvoff, (unsigned)(k * 64 * 2048 + p * 1024 + cc * 128));
    } else if (k < 8) {
      const int kk = k - 4;
      if (!(p == 1 && kk == 3 && wv >= 4))
        dma16(rw, lb + (unsigned)(XIMG + (wv + 8 * kk) * 1024), wvoff, (unsigned)(((4 * kk + p) * D + cc * 64) * (D * 2)));
    } else {
      if (wv < 2) dma16(rx, lb + (unsigned)((wv + 32) * 1024), xvoff, (unsigned)(4 * 64 * 2048 + p * 1024 + cc * 128));
    }
  };

  const int g = lane >> 4, r = lane & 15;
  unsigned fa0, fa1;
  {
    const int ch = kc * 4 + g;
    const int v0 = 16 * wm + r, v1 = v0 + 1;
    fa0 = (unsigned)(((v0 >> 1) * 16 + (v0 & 1)) * 128 + ((ch ^ (((v0 >> 1) & 3) << 1)) * 16));
    fa1 = (unsigned)(((v1 >> 1) * 16 + (v1 & 1)) * 128 + ((ch ^ (((v1 >> 1) & 3) << 1)) * 16));
  }
  const unsigned fb = (unsigned)(XIMG + (kc * 32 + 8 * g + (r >> 2)) * 64 + ((nh ^ (g & 1)) * 32) + (r & 3) * 8);

  f32x4 acc[8];
#pragma unroll
  for (int m = 0; m < 8; ++m) acc[m] = f32x4{0.f, 0.f, 0.f, 0.f};

#pragma unroll
  for (int k = 0; k < 9; ++k) issue_piece(0, k);
  asm volatile("s_waitcnt vmcnt(0)" ::: "memory");
  __builtin_amdgcn_s_barrier();

  auto stage = [&](int s, auto PC) {
    constexpr int P = decltype(PC)::value;
    constexpr int NT = 8 - P;
    LDS_AS char* base = (LDS_AS char*)smem + P * STG;
    bf16x8 F[15];
#pragma unroll
    for (int q = 0; q < 8; ++q) F[q] = *(LDS_AS bf16x8*)(base + fa0 + q * 256);
#pragma unroll
    for (int j = 0; j < NT; ++j) {
      bf16x4 lo = __builtin_amdgcn_ds_read_tr16_b64_v4bf16((LDS_AS bf16x4*)(base + fb + j * 4096));
      bf16x4 hi = __builtin_amdgcn_ds_read_tr16_b64_v4bf16((LDS_AS bf16x4*)(base + fb + j * 4096 + 256));
      bf16x8 Bf = __builtin_shufflevector(lo, hi, 0, 1, 2, 3, 4, 5, 6, 7);
      if (j + 1 < NT) F[8 + j] = *(LDS_AS bf16x8*)(base + fa1 + j * 256);
      if (s + 1 < 16) { issue_piece(s + 1, j); if (j == NT - 1) { for (int k = NT; k < 9; ++k) issue_piece(s + 1, k); } }
#pragma unroll
      for (int m = 0; m < 8; ++m)
        acc[m] = __builtin_amdgcn_mfma_f32_16x16x32_bf16(Bf, F[m + j], acc[m], 0, 0, 0);
    }
    asm volatile("s_waitcnt vmcnt(0) lgkmcnt(0)" ::: "memory");
    __builtin_amdgcn_s_barrier();
  };
  for (int s = 0; s < 16; s += 2) {
    stage(s, std::integral_constant<int, 0>{});
    stage(s + 1, std::integral_constant<int, 1>{});
  }

  {
    LDS_AS f32x4* red = (LDS_AS f32x4*)smem;
    const int pairid = wv & 3;
#pragma unroll
    for (int mm = 0; mm < 4; ++mm) {
      const int m = (kc ? 0 : 4) + mm;
      red[((pairid * 2 + kc) * 4 + mm) * 64 + lane] = kc ? acc[mm] : acc[4 + mm];
      (void)m;
    }
    __syncthreads();
    f32x4 v[4];
#pragma unroll
    for (int mm = 0; mm < 4; ++mm) {
      f32x4 o = red[((pairid * 2 + (kc ^ 1)) * 4 + mm) * 64 + lane];
      v[mm] = (kc ? acc[4 + mm] : acc[mm]) + o;
    }
    const int nbase = n0 + nh * 16 + 4 * g;
    const bf16x4 bv = *(const bf16x4*)(bias + nbase);
    const int lrow = l0 + 128 * wm + 8 * r + 4 * kc;
    f32x4 s1 = {0.f, 0.f, 0.f, 0.f};
#pragma unroll
    for (int mm = 0; mm < 4; ++mm) {
#pragma unroll
      for (int e = 0; e < 4; ++e) v[mm][e] += (float)bv[e];
      if (lrow + mm < L) {
        s1 += v[mm];
        *(f32x4*)(y + ((size_t)(b * L + lrow + mm)) * D + nbase) = v[mm];
      }
    }
    int cnt = 0;
    {
      int c = L - lrow; c = c < 0 ? 0 : (c > 4 ? 4 : c);
      float cf = row16_sum((float)c);
      cnt = (int)cf;
    }
    f32x4 mean, m2;
#pragma unroll
    for (int e = 0; e < 4; ++e) mean[e] = row16_sum(s1[e]) / (float)(cnt > 0 ? cnt : 1);
    f32x4 s2 = {0.f, 0.f, 0.f, 0.f};
#pragma unroll
    for (int mm = 0; mm < 4; ++mm) {
      if (lrow + mm < L) { f32x4 d = v[mm] - mean; s2 += d * d; }
    }
#pragma unroll
    for (int e = 0; e < 4; ++e) m2[e] = row16_sum(s2[e]);
    if (r == 0) {
      const int pi = (mt & 7) * 4 + wm * 2 + kc;
#pragma unroll
      for (int e = 0; e < 4; ++e) part[(size_t)(b * 32 + pi) * D + nbase + e] = f32x2{mean[e], m2[e]};
    }
  }
}

__device__ __forceinline__ int part_count(int pi) {
  const int base = (pi >> 2) * 256 + 128 * ((pi >> 1) & 1) + 4 * (pi & 1);
  int cnt = 0;
  for (int r = 0; r < 16; ++r) { int c = L - (base + 8 * r); c = c < 0 ? 0 : (c > 4 ? 4 : c); cnt += c; }
  return cnt;
}

__device__ __forceinline__ float gelu_tanh(float v) {
  const float u2 = v * (1.5957691216057308f + 0.07135481627159826f * v * v);
  const float e = __builtin_amdgcn_exp2f(-u2 * 1.4426950408889634f);
  return v * __builtin_amdgcn_rcpf(1.0f + e);
}

__global__ __launch_bounds__(256)
void norm_gelu_kernel(const float* __restrict__ y, const f32x2* __restrict__ part, const __bf16* __restrict__ gamma,
                      const __bf16* __restrict__ beta, __bf16* __restrict__ out) {
  __shared__ float sc[64], sh[64];
  const int bid = blockIdx.x;
  const int b = bid >> 7, cg = (bid >> 4) & 7, rb = bid & 15;
  const int tid = threadIdx.x;
  if (tid < 64) {
    const int n = cg * 64 + tid;
    float cn = 0.f, mean = 0.f, m2 = 0.f;
    for (int pi = 0; pi < 32; ++pi) {
      const float c = (float)part_count(pi);
      if (c > 0.f) {
        const f32x2 pm = part[(size_t)(b * 32 + pi) * D + n];
        const float tot = cn + c, d = pm[0] - mean;
        mean += d * (c / tot);
        m2 += pm[1] + d * d * (cn * c / tot);
        cn = tot;
      }
    }
    const float rstd = 1.0f / sqrtf(m2 / (float)L + 1e-5f);
    const float ga = (float)gamma[n], be = (float)beta[n];
    sc[tid] = rstd * ga;
    sh[tid] = be - mean * rstd * ga;
  }
  __syncthreads();
  const int c8 = (tid & 7) * 8, rr = tid >> 3;
  float s[8], h[8];
#pragma unroll
  for (int e = 0; e < 8; ++e) { s[e] = sc[c8 + e]; h[e] = sh[c8 + e]; }
  const int r0 = rb * 125;
  for (int i = rr; i < 125; i += 32) {
    const int l = r0 + i;
    if (l < L) {
      const size_t off = ((size_t)(b * L + l)) * D + cg * 64 + c8;
      const f32x4 a0 = *(const f32x4*)(y + off), a1 = *(const f32x4*)(y + off + 4);
      bf16x8 o;
#pragma unroll
      for (int e = 0; e < 4; ++e) {
        o[e] = (__bf16)gelu_tanh(a0[e] * s[e] + h[e]);
        o[4 + e] = (__bf16)gelu_tanh(a1[e] * s[4 + e] + h[4 + e]);
      }
      *(bf16x8*)(out + off) = o;
    }
  }
}

extern "C" void kernel_launch(void* const* d_in, const int* in_sizes, int n_in,
                              void* d_out, int out_size, void* d_ws, size_t ws_size,
                              hipStream_t stream) {
  const __bf16* x = (const __bf16*)d_in[0];
  const __bf16* w = (const __bf16*)d_in[1];
  const __bf16* bias = (const __bf16*)d_in[2];
  const __bf16* gamma = (const __bf16*)d_in[3];
  const __bf16* beta = (const __bf16*)d_in[4];
  float* y = (float*)d_ws;
  f32x2* part = (f32x2*)((char*)d_ws + (size_t)16 * 1024 * 1024);
  conv_gemm_kernel<<<256, 512, 0, stream>>>(x, w, bias, y, part);
  norm_gelu_kernel<<<256, 256, 0, stream>>>(y, part, gamma, beta, (__bf16*)d_out);
}
```

```cpp
#include <hip/hip_runtime.h>
#include <hip/hip_bf16.h>
#include <type_traits>

typedef __bf16 bf16x4 __attribute__((ext_vector_type(4)));
typedef __bf16 bf16x8 __attribute__((ext_vector_type(8)));
typedef float f32x4 __attribute__((ext_vector_type(4)));
typedef float f32x2 __attribute__((ext_vector_type(2)));
typedef unsigned u32x2 __attribute__((ext_vector_type(2)));
typedef unsigned u32x4 __attribute__((ext_vector_type(4)));
#define LDS_AS __attribute__((address_space(3)))
#define DI __device__ __forceinline__

constexpr int NB = 2, T = 4000, D = 512, KT = 15, L = 1993;
constexpr int BM = 128, BN = 64;
constexpr int XIMG = 136 * 128;
constexpr int WBASE = 2 * XIMG;
constexpr int WPOS0 = 0, WPOS1 = 32768, WPOS2 = 65536, WPOS3 = 65536 + 28672;
constexpr int FLAGS = WBASE + 65536 + 57344;
constexpr int PROG = FLAGS + 64;
constexpr int PFSCR = FLAGS + 256;
constexpr int LDS_BYTES = FLAGS + 1024;
constexpr int SPIN_CAP = 1 << 22;
#define POLL_SLEEP 1
#define PF_DIST 2

DI void dma16(__amdgpu_buffer_rsrc_t rs, unsigned ldsaddr, unsigned voff, unsigned soff) {
  asm volatile("s_mov_b32 m0, %0\n\ts_nop 0\n\tbuffer_load_dwordx4 %1, %2, %3 offen lds"
               :: "s"(ldsaddr), "v"(voff), "s"(rs), "s"(soff) : "memory");
}
DI void dma4(__amdgpu_buffer_rsrc_t rs, unsigned ldsaddr, unsigned voff, unsigned soff) {
  asm volatile("s_mov_b32 m0, %0\n\ts_nop 0\n\tbuffer_load_dword %1, %2, %3 offen lds"
               :: "s"(ldsaddr), "v"(voff), "s"(rs), "s"(soff) : "memory");
}
#define WAIT_VM(n) asm volatile("s_waitcnt vmcnt(" #n ")" ::: "memory")
#define CBAR() asm volatile("" ::: "memory")
#define SCHED() __builtin_amdgcn_sched_barrier(0)

DI float row16_sum(float v) {
  v += __builtin_bit_cast(float, __builtin_amdgcn_update_dpp(0, __builtin_bit_cast(int, v), 0xB1, 0xF, 0xF, true));
  v += __builtin_bit_cast(float, __builtin_amdgcn_update_dpp(0, __builtin_bit_cast(int, v), 0x4E, 0xF, 0xF, true));
  v += __builtin_bit_cast(float, __builtin_amdgcn_update_dpp(0, __builtin_bit_cast(int, v), 0x141, 0xF, 0xF, true));
  v += __builtin_bit_cast(float, __builtin_amdgcn_update_dpp(0, __builtin_bit_cast(int, v), 0x140, 0xF, 0xF, true));
  return v;
}

#define STAMP(k) do {} while (0)

__global__ __launch_bounds__(512, 2)
void conv_gemm_kernel(const __bf16* __restrict__ x, const __bf16* __restrict__ w, const __bf16* __restrict__ bias,
                      float* __restrict__ y, f32x2* __restrict__ part, unsigned long long* __restrict__ stamps) {
  __shared__ __attribute__((aligned(1024))) char smem[LDS_BYTES];
  LDS_AS char* const lds = (LDS_AS char*)smem;
  const int tid = threadIdx.x, lane = tid & 63;
  const int wv = __builtin_amdgcn_readfirstlane(tid >> 6);
  const int bid = blockIdx.x;
  const int xcd = bid & 7, idx = bid >> 3;
  const int mt = (xcd & 1) * 16 + (idx & 15), nt = (xcd >> 1) * 2 + (idx >> 4);
  const int b = mt >> 4, l0 = (mt & 15) * BM, n0 = nt * BN;
  const unsigned sbase = (unsigned)(size_t)lds;
  volatile LDS_AS unsigned* const flagw = (volatile LDS_AS unsigned*)(lds + FLAGS);
  volatile LDS_AS unsigned* const progw = (volatile LDS_AS unsigned*)(lds + PROG);
  STAMP(0);

  const __amdgpu_buffer_rsrc_t rx = __builtin_amdgcn_make_buffer_rsrc((void*)x, 0, NB * T * D * 2, 0x00020000);
  const __amdgpu_buffer_rsrc_t rw = __builtin_amdgcn_make_buffer_rsrc((void*)w, 0, KT * D * D * 2, 0x00020000);

  auto wait_prog = [&](unsigned need) {
    for (int sp = 0; sp < SPIN_CAP; ++sp) {
      const u32x4 pv = *(volatile LDS_AS u32x4*)(lds + PROG);
      const unsigned mn = min(min(pv[0], pv[1]), min(pv[2], pv[3]));
      if (__builtin_amdgcn_readfirstlane(mn) >= need) break;
      __builtin_amdgcn_s_sleep(POLL_SLEEP);
    }
    CBAR();
  };

  if (wv >= 4) {
    if (wv < 6) {
      const int h = wv - 4;
      unsigned wvo[2];
#pragma unroll
      for (int e = 0; e < 2; ++e) {
        const int f = ((lane >> 4) & 1) | (e << 1);
        wvo[e] = (unsigned)(((2 * h + e) * 8 + (lane >> 3)) * (D * 2) + (((lane & 7) ^ (f << 1)) * 16) + n0 * 2);
      }
      auto issue_slot = [&](int cc, auto SLC) {
        constexpr int SL = decltype(SLC)::value;
        constexpr int P = SL >> 1, KC = SL & 1, NT = 8 - P;
        constexpr int WP = SL == 0 ? WPOS0 : SL == 1 ? WPOS1 : SL == 2 ? WPOS2 : WPOS3;
        const unsigned lb = sbase + (unsigned)(WBASE + WP + 2 * h * 1024);
        const unsigned so = (unsigned)((P * D + cc * 64 + KC * 32) * (D * 2));
#pragma unroll
        for (int j = 0; j < NT; ++j) {
          dma16(rw, lb + j * 4096, wvo[0], so + j * (2 * D * D * 2));
          dma16(rw, lb + j * 4096 + 1024, wvo[1], so + j * (2 * D * D * 2));
        }
      };
      issue_slot(0, std::integral_constant<int, 0>{});
      issue_slot(0, std::integral_constant<int, 1>{});
      issue_slot(0, std::integral_constant<int, 2>{});
      issue_slot(0, std::integral_constant<int, 3>{});
      __builtin_amdgcn_s_barrier();
      WAIT_VM(44); flagw[h] = 1;
      WAIT_VM(28); flagw[h] = 2;
      WAIT_VM(14); flagw[h] = 3;
      for (int cc = 1; cc < 8; ++cc) {
        const unsigned i = cc * 4;
        wait_prog(i - 3); issue_slot(cc, std::integral_constant<int, 0>{}); WAIT_VM(16); flagw[h] = i;
        wait_prog(i - 2); issue_slot(cc, std::integral_constant<int, 1>{}); WAIT_VM(16); flagw[h] = i + 1;
        wait_prog(i - 1); issue_slot(cc, std::integral_constant<int, 2>{}); WAIT_VM(14); flagw[h] = i + 2;
        wait_prog(i);     issue_slot(cc, std::integral_constant<int, 3>{}); WAIT_VM(14); flagw[h] = i + 3;
      }
      WAIT_VM(0); flagw[h] = 32;
    } else if (wv == 6) {
      unsigned xvo[8];
#pragma unroll
      for (int e = 0; e < 8; ++e)
        xvo[e] = (unsigned)((b * T + 2 * l0 + 2 * ((lane >> 3) ^ (e & 1))) * (D * 2) + (((lane & 7) ^ (((e >> 1) & 3) << 1)) * 16));
      auto issue_x = [&](int S) {
        const unsigned lb = sbase + (unsigned)((S & 1) * XIMG);
        const unsigned so = (unsigned)((S & 1) * 1024 + (S >> 1) * 128);
#pragma unroll
        for (int i = 0; i < 17; ++i) dma16(rx, lb + i * 1024, xvo[i & 7], so + i * 16384);
      };
      issue_x(0); issue_x(1);
      __builtin_amdgcn_s_barrier();
      WAIT_VM(17); flagw[2] = 2;
      WAIT_VM(0); flagw[2] = 4;
      for (int S = 2; S < 16; ++S) {
        wait_prog(2 * S - 2); issue_x(S); WAIT_VM(0); flagw[2] = 2 * S + 2;
      }
    } else {
      if (lane < 8) { flagw[lane] = 0; progw[lane] = 0; }
      asm volatile("s_waitcnt lgkmcnt(0)" ::: "memory");
      __builtin_amdgcn_s_barrier();
      const unsigned xpf = (unsigned)((b * T + 2 * l0 + 2 * ((nt & 1) * 64 + lane)) * (D * 2));
      const unsigned wpf = lane < 60 ? (unsigned)(((lane >> 2) * D + (mt & 15) * 4 + (lane & 3)) * (D * 2) + n0 * 2) : 0x80000000u;
      for (int cc = 1; cc < 8; ++cc) {
        if (cc >= PF_DIST + 1) wait_prog(4 * (cc - PF_DIST) - 3);
        dma4(rw, sbase + PFSCR, wpf, (unsigned)(cc * 64 * D * 2));
        dma4(rx, sbase + PFSCR, xpf, (unsigned)(cc * 128));
        dma4(rx, sbase + PFSCR, xpf, (unsigned)(cc * 128 + 1024));
      }
    }
    STAMP(3);
    return;
  }

  const int nq = wv;
  const int g = lane >> 4, r = lane & 15;
  unsigned fa[2][2][2];
#pragma unroll
  for (int hi = 0; hi < 2; ++hi)
#pragma unroll
    for (int od = 0; od < 2; ++od)
#pragma unroll
      for (int kc = 0; kc < 2; ++kc) {
        const int v = r + hi;
        const int pos = (kc * 4 + g) ^ (((v >> 1) & 3) << 1);
        fa[hi][od][kc] = (unsigned)((8 * v + (od ? -(v & 1) : (v & 1))) * 128 + pos * 16);
      }
  unsigned fb;
  {
    const int q4 = r >> 2, p4 = r & 3;
    const int f = ((q4 >> 1) & 1) | ((g & 1) << 1);
    fb = (unsigned)(WBASE + (8 * g + q4) * 128 + ((nq ^ f) * 32) + p4 * 8);
  }
  auto ldF = [&](int SL, int q) -> bf16x8 {
    return *(LDS_AS bf16x8*)(lds + fa[q >> 3][q & 1][SL & 1] + (SL >> 1) * XIMG + (q & 7) * 128);
  };
  auto ldB = [&](int SL, int j) -> bf16x8 {
    const int WP = SL == 0 ? WPOS0 : SL == 1 ? WPOS1 : SL == 2 ? WPOS2 : WPOS3;
    bf16x4 lo = __builtin_amdgcn_ds_read_tr16_b64_v4bf16((LDS_AS bf16x4*)(lds + fb + WP + j * 4096));
    bf16x4 hi = __builtin_amdgcn_ds_read_tr16_b64_v4bf16((LDS_AS bf16x4*)(lds + fb + WP + j * 4096 + 512));
    return __builtin_shufflevector(lo, hi, 0, 1, 2, 3, 4, 5, 6, 7);
  };
  auto wait_full_slow = [&](unsigned need) {
    for (int sp = 0; sp < SPIN_CAP; ++sp) {
      const u32x4 fv = *(volatile LDS_AS u32x4*)(lds + FLAGS);
      const unsigned mn = min(min(fv[0], fv[1]), fv[2]);
      if (__builtin_amdgcn_readfirstlane(mn) >= need) break;
    }
  };

  f32x4 acc[8];
#pragma unroll
  for (int m = 0; m < 8; ++m) acc[m] = f32x4{0.f, 0.f, 0.f, 0.f};

  __builtin_amdgcn_s_barrier();
  wait_full_slow(1);
  CBAR();
  STAMP(1);
  bf16x8 G[8], B0, B1;
#pragma unroll
  for (int q = 0; q < 8; ++q) G[q] = ldF(0, q);
  B0 = ldB(0, 0); B1 = ldB(0, 1);

  auto slot = [&](unsigned i, auto SLC) {
    constexpr int SL = decltype(SLC)::value;
    constexpr int NT = 8 - (SL >> 1);
    constexpr int SN = (SL + 1) & 3;
    bf16x8 F[15];
#pragma unroll
    for (int q = 0; q < 8; ++q) F[q] = G[q];
    u32x4 fv;
#pragma unroll
    for (int j = 0; j < NT; ++j) {
      bf16x4 lo, hi;
      constexpr int WPC = SL == 0 ? WPOS0 : SL == 1 ? WPOS1 : SL == 2 ? WPOS2 : WPOS3;
      constexpr int WPN = SN == 0 ? WPOS0 : SN == 1 ? WPOS1 : SN == 2 ? WPOS2 : WPOS3;
      const int boff = (j + 2 < NT) ? WPC + (j + 2) * 4096 : WPN + (j + 2 - NT) * 4096;
      const bool doG = j >= NT - 4;
      const int gq = 2 * (j - (NT - 4));
#define MF(m) acc[m] = __builtin_amdgcn_mfma_f32_16x16x32_bf16(B0, F[(m) + j], acc[m], 0, 0, 0)
      SCHED();
      MF(0);
      SCHED();
      if (j == NT - 4) {
        const unsigned need = min(i + 2u, 32u);
        const unsigned mn = min(min(fv[0], fv[1]), fv[2]);
        if (__builtin_amdgcn_readfirstlane(mn) < need) wait_full_slow(need);
        CBAR();
      }
      if (j + 1 < NT) F[8 + j] = ldF(SL, 8 + j); else progw[nq] = i + 1;
      SCHED();
      MF(1);
      SCHED();
      lo = __builtin_amdgcn_ds_read_tr16_b64_v4bf16((LDS_AS bf16x4*)(lds + fb + boff));
      SCHED();
      MF(2);
      SCHED();
      hi = __builtin_amdgcn_ds_read_tr16_b64_v4bf16((LDS_AS bf16x4*)(lds + fb + boff + 512));
      SCHED();
      MF(3);
      SCHED();
      if (doG) G[gq] = ldF(SN, gq);
      if (j == NT - 6) fv = *(volatile LDS_AS u32x4*)(lds + FLAGS);
      SCHED();
      MF(4);
      SCHED();
      MF(5);
      SCHED();
      if (doG) G[gq + 1] = ldF(SN, gq + 1);
      SCHED();
      MF(6);
      SCHED();
      MF(7);
      B0 = B1; B1 = __builtin_shufflevector(lo, hi, 0, 1, 2, 3, 4, 5, 6, 7);
    }
  };
  for (unsigned i = 0; i < 32; i += 4) {
    slot(i, std::integral_constant<int, 0>{});
    slot(i + 1, std::integral_constant<int, 1>{});
    slot(i + 2, std::integral_constant<int, 2>{});
    slot(i + 3, std::integral_constant<int, 3>{});
  }
  STAMP(2);

  {
    const int nbase = n0 + nq * 16 + 4 * g;
    const bf16x4 bv = *(const bf16x4*)(bias + nbase);
    const int lrow = l0 + 8 * r;
    f32x4 s1 = {0.f, 0.f, 0.f, 0.f};
#pragma unroll
    for (int m = 0; m < 8; ++m) {
#pragma unroll
      for (int e = 0; e < 4; ++e) acc[m][e] += (float)bv[e];
      if (lrow + m < L) {
        s1 += acc[m];
        *(f32x4*)(y + ((size_t)(b * L + lrow + m)) * D + nbase) = acc[m];
      }
    }
    const int cnt = min(L - l0, BM);
    const float inv = 1.0f / (float)cnt;
    f32x4 mean, m2;
#pragma unroll
    for (int e = 0; e < 4; ++e) mean[e] = row16_sum(s1[e]) * inv;
    f32x4 s2 = {0.f, 0.f, 0.f, 0.f};
#pragma unroll
    for (int m = 0; m < 8; ++m) {
      if (lrow + m < L) { f32x4 d = acc[m] - mean; s2 += d * d; }
    }
#pragma unroll
    for (int e = 0; e < 4; ++e) m2[e] = row16_sum(s2[e]);
    if (r == 0) {
#pragma unroll
      for (int e = 0; e < 4; ++e) part[(size_t)mt * D + nbase + e] = f32x2{mean[e], m2[e]};
    }
  }
  STAMP(3);
}

DI float gelu_tanh(float v) {
  const float u2 = v * (1.5957691216057308f + 0.07135481627159826f * v * v);
  const float e = __builtin_amdgcn_exp2f(-u2 * 1.4426950408889634f);
  return v * __builtin_amdgcn_rcpf(1.0f + e);
}

__global__ __launch_bounds__(256)
void norm_gelu_kernel(const float* __restrict__ y, const f32x2* __restrict__ part, const __bf16* __restrict__ gamma,
                      const __bf16* __restrict__ beta, __bf16* __restrict__ out) {
  __shared__ float sc[64], sh[64];
  const int bid = blockIdx.x;
  const int b = bid >> 8, cg = (bid >> 5) & 7, rb = bid & 31;
  const int tid = threadIdx.x;
  const int c8 = (tid & 7) * 8, rr = tid >> 3;
  const int r0 = rb * 63;
  f32x4 a[2][2];
#pragma unroll
  for (int k = 0; k < 2; ++k) {
    const int i = rr + 32 * k, l = min(r0 + i, L - 1);
    const size_t off = ((size_t)(b * L + l)) * D + cg * 64 + c8;
    a[k][0] = *(const f32x4*)(y + off); a[k][1] = *(const f32x4*)(y + off + 4);
  }
  if (tid < 64) {
    const int n = cg * 64 + tid;
    f32x2 pm[16];
#pragma unroll
    for (int pi = 0; pi < 16; ++pi) pm[pi] = part[(size_t)(b * 16 + pi) * D + n];
    float mean = 0.f;
#pragma unroll
    for (int pi = 0; pi < 16; ++pi) mean += pm[pi][0] * (pi < 15 ? 128.f : 73.f);
    mean *= (1.0f / (float)L);
    float m2 = 0.f;
#pragma unroll
    for (int pi = 0; pi < 16; ++pi) { const float d = pm[pi][0] - mean; m2 += pm[pi][1] + d * d * (pi < 15 ? 128.f : 73.f); }
    const float rstd = 1.0f / sqrtf(m2 / (float)L + 1e-5f);
    const float ga = (float)gamma[n], be = (float)beta[n];
    sc[tid] = rstd * ga;
    sh[tid] = be - mean * rstd * ga;
  }
  __syncthreads();
  float s[8], h[8];
#pragma unroll
  for (int e = 0; e < 8; ++e) { s[e] = sc[c8 + e]; h[e] = sh[c8 + e]; }
#pragma unroll
  for (int k = 0; k < 2; ++k) {
    const int i = rr + 32 * k, l = r0 + i;
    if (i < 63 && l < L) {
      const size_t off = ((size_t)(b * L + l)) * D + cg * 64 + c8;
      bf16x8 o;
#pragma unroll
      for (int e = 0; e < 4; ++e) {
        o[e] = (__bf16)gelu_tanh(a[k][0][e] * s[e] + h[e]);
        o[4 + e] = (__bf16)gelu_tanh(a[k][1][e] * s[4 + e] + h[4 + e]);
      }
      *(bf16x8*)(out + off) = o;
    }
  }
}

extern "C" void kernel_launch(void* const* d_in, const int* in_sizes, int n_in,
                              void* d_out, int out_size, void* d_ws, size_t ws_size,
                              hipStream_t stream) {
  const __bf16* x = (const __bf16*)d_in[0];
  const __bf16* w = (const __bf16*)d_in[1];
  const __bf16* bias = (const __bf16*)d_in[2];
  const __bf16* gamma = (const __bf16*)d_in[3];
  const __bf16* beta = (const __bf16*)d_in[4];
  float* y = (float*)d_ws;
  f32x2* part = (f32x2*)((char*)d_ws + (size_t)16 * 1024 * 1024);
  unsigned long long* stamps = (unsigned long long*)((char*)d_ws + (size_t)32 * 1024 * 1024);
  conv_gemm_kernel<<<256, 512, 0, stream>>>(x, w, bias, y, part, stamps);
  norm_gelu_kernel<<<512, 256, 0, stream>>>(y, part, gamma, beta, (__bf16*)d_out);
}
```

```cpp
#include <hip/hip_runtime.h>
#include <type_traits>

typedef __bf16 bf16x4 __attribute__((ext_vector_type(4)));
typedef __bf16 bf16x8 __attribute__((ext_vector_type(8)));
typedef float f32x4 __attribute__((ext_vector_type(4)));
typedef float f32x2 __attribute__((ext_vector_type(2)));
typedef unsigned u32x2 __attribute__((ext_vector_type(2)));
typedef unsigned u32x4 __attribute__((ext_vector_type(4)));
#define LDS_AS __attribute__((address_space(3)))
#define GAS __attribute__((address_space(1)))
#define DI __device__ __forceinline__

constexpr int NB = 2, T = 4000, D = 512, KT = 15, L = 1993;
constexpr int XIMG = 136 * 128;
constexpr int WREG = 2 * XIMG;
constexpr int LDS_BYTES = 4 * WREG;
constexpr int SPIN_CAP = 1 << 20;
constexpr unsigned INVALID = 0xFFFFFFFFu;
constexpr int GRAN_BYTES = 256 * 64 * 8;

#define RLX_AGENT __ATOMIC_RELAXED, __HIP_MEMORY_SCOPE_AGENT
#define WAIT_VM(n) asm volatile("s_waitcnt vmcnt(" #n ")" ::: "memory")
#define SCHED() __builtin_amdgcn_sched_barrier(0)
#define PF1 6
#define PF2 20
#define XD 4
constexpr unsigned woff(int S) {
  return S < 16 ? (unsigned)((2 * (S & 7)) * (D * D * 2) + (S >> 3) * 32768)
                : (unsigned)((2 * ((S - 16) % 7) + 1) * (D * D * 2) + ((S - 16) / 7) * 32768);
}

template <unsigned long long MASK>
DI unsigned xsel(unsigned oth, unsigned own) {
  unsigned r;
  register unsigned long long m asm("vcc") = MASK;
  asm volatile("v_cndmask_b32_dpp %0, %1, %2, vcc quad_perm:[1,0,3,2] row_mask:0xf bank_mask:0xf" : "=v"(r) : "v"(oth), "v"(own), "s"(m));
  return r;
}
DI void mfma(f32x4& acc, const u32x4& a, const bf16x8& b) {
  asm volatile("v_mfma_f32_16x16x32_bf16 %0, %1, %2, %0" : "+a"(acc) : "v"(a), "v"(b));
}

DI float row16_sum(float v) {
  v += __builtin_bit_cast(float, __builtin_amdgcn_update_dpp(0, __builtin_bit_cast(int, v), 0xB1, 0xF, 0xF, true));
  v += __builtin_bit_cast(float, __builtin_amdgcn_update_dpp(0, __builtin_bit_cast(int, v), 0x4E, 0xF, 0xF, true));
  v += __builtin_bit_cast(float, __builtin_amdgcn_update_dpp(0, __builtin_bit_cast(int, v), 0x141, 0xF, 0xF, true));
  v += __builtin_bit_cast(float, __builtin_amdgcn_update_dpp(0, __builtin_bit_cast(int, v), 0x140, 0xF, 0xF, true));
  return v;
}
DI float gelu_tanh(float v) {
  const float t = v * (-2.3022081985f - 0.1029432397f * v * v);
  const float e = __builtin_amdgcn_exp2f(t);
  return v * __builtin_amdgcn_rcpf(1.0f + e);
}

#define STAMP(k) do {} while (0)

__global__ __launch_bounds__(256, 1)
void conv_gn_gelu_kernel(const __bf16* __restrict__ x, const __bf16* __restrict__ w, const __bf16* __restrict__ bias,
                         const __bf16* __restrict__ gamma, const __bf16* __restrict__ beta, __bf16* __restrict__ out,
                         unsigned long long* gran, unsigned long long* __restrict__ stamps) {
  __shared__ __attribute__((aligned(1024))) char smem[LDS_BYTES];
  LDS_AS char* const lds = (LDS_AS char*)smem;
  const int tid = threadIdx.x, lane = tid & 63;
  const int wv = __builtin_amdgcn_readfirstlane(tid >> 6);
  const int bid = blockIdx.x;
  const int xcd = bid & 7, idx = bid >> 3;
  const int mt = (xcd & 1) * 16 + (idx & 15), nt = (xcd >> 1) * 2 + (idx >> 4);
  const int b = mt >> 4, l0 = (mt & 15) * 128, n0 = nt * 64;
  const int c = lane & 15, g = lane >> 4;
  STAMP(0);

  const __amdgpu_buffer_rsrc_t rx = __builtin_amdgcn_make_buffer_rsrc((void*)x, 0, NB * T * D * 2, 0x00020000);
  const __amdgpu_buffer_rsrc_t rw = __builtin_amdgcn_make_buffer_rsrc((void*)w, 0, KT * D * D * 2, 0x00020000);
  LDS_AS char* const myl = lds + wv * WREG;

  const int grp = b * 8 + nt;
  GAS unsigned long long* const gown = (GAS unsigned long long*)gran + ((grp * 16 + (mt & 15)) * 64 + wv * 16);
  GAS unsigned long long* const mbase = (GAS unsigned long long*)gran + GRAN_BYTES / 8 + (grp * 4 + wv) * 256;
  GAS unsigned long long* const min_ = mbase + (mt & 15) * 16 + c;
  GAS unsigned long long* const mout = mbase + c * 16 + (mt & 15);
  __hip_atomic_store(gown + c, ~0ull, RLX_AGENT);
  unsigned long long hr = __hip_atomic_load(min_, RLX_AGENT);
  SCHED();

  const unsigned wo = (unsigned)((8 * g + 4 * (c & 1)) * 1024 + n0 * 2 + (c >> 1) * 16);
  u32x4 raw[5][4];
  auto ldW = [&](int slot, int i, unsigned soff) {
    raw[slot][i] = __builtin_bit_cast(u32x4, __builtin_amdgcn_raw_buffer_load_b128(rw, wo + i * 1024, soff, 0));
  };
  unsigned wb = (unsigned)(wv * 65536);
#pragma unroll
  for (int i = 0; i < 4; ++i) ldW(0, i, wb + woff(0));
  SCHED();

  const unsigned xo0 = (unsigned)((b * T + 2 * l0 + 16 * (lane >> 3)) * (D * 2) + (((lane & 7) ^ (((lane >> 4) & 3) << 1)) * 16));
  const unsigned xo1 = (unsigned)((b * T + 2 * l0 + 256 + 2 * (lane >> 3)) * (D * 2) + (lane & 7) * 16);
  LDS_AS char* const xa0 = myl + lane * 16;
  LDS_AS char* const xa1 = myl + (17 * (lane >> 3) + 16) * 128 + (lane & 7) * 16;
  auto ldX = [&](int pc, unsigned so) -> u32x4 {
    return __builtin_bit_cast(u32x4, __builtin_amdgcn_raw_buffer_load_b128(rx, pc < 16 ? xo0 : xo1,
                                                                           so + (pc < 16 ? (pc >> 1) * 2048 + (pc & 1) * 131072 : 0), 0));
  };
  auto stX = [&](int p, int pc, const u32x4& v) {
    *(LDS_AS u32x4*)((pc < 16 ? xa0 : xa1) + p * XIMG + (pc < 16 ? (17 * (pc >> 1) + 8 * (pc & 1)) * 128 : 0)) = v;
  };
  u32x4 xs[17];
#pragma unroll
  for (int i = 0; i < 17; ++i) xs[i] = ldX(i, (unsigned)(wv * 128));
  SCHED();
#pragma unroll
  for (int s = 1; s < 5; ++s)
#pragma unroll
    for (int i = 0; i < 4; ++i) ldW(s, i, wb + woff(s));
  SCHED();

  const unsigned pfw = lane < 32 ? (unsigned)(((lane >> 2) * 2 * D + (mt & 15) * 4 + (lane & 3)) * (D * 2) + n0 * 2) : 0x80000000u;
  unsigned pf[1];
  pf[0] = __builtin_amdgcn_raw_buffer_load_b32(rw, pfw, wb, 0);
  SCHED();

  unsigned fa[2][2];
#pragma unroll
  for (int hi = 0; hi < 2; ++hi) {
    const int v = c + hi;
    fa[0][hi] = (unsigned)(wv * WREG + v * 128 + ((g ^ (((v >> 1) & 3) << 1)) * 16));
    fa[1][hi] = fa[0][hi] ^ 64u;
  }
  auto ldF = [&](int pass, int q) -> bf16x8 {
    return *(LDS_AS bf16x8*)(lds + fa[pass & 1][q >> 3] + (pass >> 1) * XIMG + (q & 7) * 2176);
  };
  unsigned U[4][2], V[4][2];
  auto mkU = [&](int slot, int i) { U[i >> 1][i & 1] = xsel<0xAAAAAAAAAAAAAAAAull>(raw[slot][i >> 1][i & 1], raw[slot][i >> 1][2 + (i & 1)]); };
  auto mkV = [&](int slot, int i) { V[i >> 1][i & 1] = xsel<0x5555555555555555ull>(raw[slot][i >> 1][2 + (i & 1)], raw[slot][i >> 1][i & 1]); };
  auto mkB = [&](int e, int d) -> unsigned {
    const unsigned sel = (e & 1) ? 0x07060302u : 0x05040100u;
    return d < 2 ? __builtin_amdgcn_perm(V[2 * d + 1][e >> 1], V[2 * d][e >> 1], sel)
                 : __builtin_amdgcn_perm(U[2 * d - 3][e >> 1], U[2 * d - 4][e >> 1], sel);
  };

  const unsigned hch = (unsigned)(hr >> 32) + 1u;
  __hip_atomic_store(mout, (hr << 32) | hch, RLX_AGENT);
  SCHED();

  f32x4 acc[8][4];
#pragma unroll
  for (int m = 0; m < 8; ++m)
#pragma unroll
    for (int e = 0; e < 4; ++e) acc[m][e] = f32x4{0.f, 0.f, 0.f, 0.f};

  bf16x8 F[2][15];
  u32x4 Bc[4], Bn[4];
#pragma unroll
  for (int i = 0; i < 8; ++i) mkU(0, i);
#pragma unroll
  for (int i = 0; i < 8; ++i) mkV(0, i);
#pragma unroll
  for (int e = 0; e < 4; ++e)
#pragma unroll
    for (int d = 0; d < 4; ++d) Bc[e][d] = mkB(e, d);
  SCHED();
#pragma unroll
  for (int i = 0; i < 4; ++i) ldW(0, i, wb + woff(5));
  SCHED();
#pragma unroll
  for (int i = 0; i < 17; ++i) stX(0, i, xs[i]);
  STAMP(1);
#pragma unroll
  for (int q = 0; q < 8; ++q) F[0][q] = ldF(0, q);

  for (unsigned t = 0; t < 2; ++t) {
    const unsigned wn = t ? 0x40000000u : wb + 262144u;
    const unsigned xs1 = (unsigned)(wv * 128 + 1024) + t * 512u, xs0 = t ? 0x40000000u : (unsigned)(wv * 128 + 512);
    auto step = [&](auto SC) {
      constexpr int S = decltype(SC)::value;
      constexpr int PS = S < 8 ? 0 : S < 16 ? 1 : S < 23 ? 2 : 3;
      constexpr int J = S - (PS == 0 ? 0 : PS == 1 ? 8 : PS == 2 ? 16 : 23), NT = PS < 2 ? 8 : 7;
      constexpr int SL = (S + 1) % 5;
      constexpr int XL = S <= 8 ? 2 * S : (S >= 14 && S <= 22) ? 2 * (S - 14) : -1;
      constexpr int XW = (S >= XD && S <= 8 + XD) ? 2 * (S - XD) : (S >= 14 + XD && S <= 22 + XD) ? 2 * (S - 14 - XD) : -1;
      const unsigned so = (S + 6 < 30) ? wb + woff(S + 6) : wn + woff(S + 6 - 30);
#pragma unroll
      for (int k = 0; k < 32; ++k) {
        const int e = k >> 3, m = k & 7;
        SCHED();
        mfma(acc[m][e], Bc[e], F[PS & 1][m + J]);
        SCHED();
        if (k < 8) mkU(SL, k);
        else if (k < 16) mkV(SL, k - 8);
        else Bn[(k - 16) >> 2][k & 3] = mkB((k - 16) >> 2, k & 3);
        if (k == 1 && J + 1 < NT) F[PS & 1][8 + J] = ldF(PS, 8 + J);
        if ((k == 3 || k == 5) && J >= NT - 4) F[(PS + 1) & 1][2 * (J - NT + 4) + (k == 5)] = ldF((PS + 1) & 3, 2 * (J - NT + 4) + (k == 5));
        if (k == 7 && XW >= 0) stX(S < 14, XW, xs[XW]);
        if (k == 10 && XW >= 0 && XW < 16) stX(S < 14, XW + 1, xs[XW + 1]);
        if (k == 13 && XL >= 0) xs[XL] = ldX(XL, S < 14 ? xs1 : xs0);
        if (k == 28 && XL >= 0 && XL < 16) xs[XL + 1] = ldX(XL + 1, S < 14 ? xs1 : xs0);
        if (k >= 16 && k < 26 && (k - 16) % 3 == 0) ldW(SL, (k - 16) / 3, so);
        if (k == 30 && (S == PF1 || S == PF2)) {
          asm volatile("" :: "v"(pf[0]));
          pf[0] = __builtin_amdgcn_raw_buffer_load_b32(rw, pfw, S == PF1 ? wb + D * D * 2 : wn, 0);
        }
      }
#pragma unroll
      for (int e = 0; e < 4; ++e) Bc[e] = Bn[e];
    };
#define ST(n) step(std::integral_constant<int, n>{})
    ST(0); ST(1); ST(2); ST(3); ST(4); ST(5); ST(6); ST(7); ST(8); ST(9); ST(10); ST(11); ST(12); ST(13); ST(14);
    ST(15); ST(16); ST(17); ST(18); ST(19); ST(20); ST(21); ST(22); ST(23); ST(24); ST(25); ST(26); ST(27); ST(28); ST(29);
    wb = wn;
  }
  asm volatile("s_nop 7\n\ts_nop 3" :: "v"(pf[0]) : "memory");
  STAMP(2);

  hr = __hip_atomic_load(min_, RLX_AGENT);
#pragma unroll
  for (int m = 0; m < 8; ++m)
#pragma unroll
    for (int e = 0; e < 4; ++e) *(LDS_AS f32x4*)(myl + ((m * 4 + e) * 64 + lane) * 16) = acc[m][e];
  const int nb = n0 + wv * 16;
  float bv[4];
#pragma unroll
  for (int i = 0; i < 4; ++i) bv[i] = (float)bias[nb + 4 * i + g];
  const bf16x4 gav = *(const bf16x4*)(gamma + nb + 4 * g), bev = *(const bf16x4*)(beta + nb + 4 * g);
  __syncthreads();
  f32x4 v[8];
#pragma unroll
  for (int m = 0; m < 8; ++m) {
    const LDS_AS char* p = lds + ((m * 4 + g) * 64 + wv * 16 + c) * 16;
    const f32x4 p0 = *(const LDS_AS f32x4*)(p), p1 = *(const LDS_AS f32x4*)(p + WREG);
    const f32x4 p2 = *(const LDS_AS f32x4*)(p + 2 * WREG), p3 = *(const LDS_AS f32x4*)(p + 3 * WREG);
    v[m] = (p0 + p1) + (p2 + p3);
#pragma unroll
    for (int i = 0; i < 4; ++i) v[m][i] += bv[i];
  }
  STAMP(3);
  const int lrow = l0 + 8 * c;
  {
    const float inv = 1.0f / (float)min(L - l0, 128);
    f32x4 s1 = {0.f, 0.f, 0.f, 0.f}, s2 = {0.f, 0.f, 0.f, 0.f}, mean;
#pragma unroll
    for (int m = 0; m < 8; ++m) if (lrow + m < L) s1 += v[m];
#pragma unroll
    for (int i = 0; i < 4; ++i) mean[i] = row16_sum(s1[i]) * inv;
#pragma unroll
    for (int m = 0; m < 8; ++m) if (lrow + m < L) { const f32x4 d = v[m] - mean; s2 += d * d; }
#pragma unroll
    for (int i = 0; i < 4; ++i) s2[i] = row16_sum(s2[i]);
    const float pm = c == 0 ? mean[0] : c == 1 ? mean[1] : c == 2 ? mean[2] : mean[3];
    const float ps = c == 0 ? s2[0] : c == 1 ? s2[1] : c == 2 ? s2[2] : s2[3];
    unsigned us = __builtin_bit_cast(unsigned, ps);
    if (us == INVALID) us = 0x7FC00000u;
    __hip_atomic_store(mout, (hr << 32) | hch, RLX_AGENT);
    if (c < 4) __hip_atomic_store(gown + 4 * c + g, ((unsigned long long)us << 32) | __builtin_bit_cast(unsigned, pm), RLX_AGENT);
  }
#pragma unroll
  for (int m = 0; m < 8; ++m) {
    u32x4 a = __builtin_bit_cast(u32x4, v[m]);
    auto r02 = __builtin_amdgcn_permlane32_swap(a[0], a[2], false, false);
    auto r13 = __builtin_amdgcn_permlane32_swap(a[1], a[3], false, false);
    auto r01 = __builtin_amdgcn_permlane16_swap(r02[0], r13[0], false, false);
    auto r23 = __builtin_amdgcn_permlane16_swap(r02[1], r13[1], false, false);
    v[m] = __builtin_bit_cast(f32x4, u32x4{r01[0], r01[1], r23[0], r23[1]});
  }
  float sc[4], sh[4];
  {
    const __amdgpu_buffer_rsrc_t rg = __builtin_amdgcn_make_buffer_rsrc((void*)gran, 0, GRAN_BYTES, 0x00020000);
    const unsigned go = (unsigned)(((grp * 16 + c) * 64 + wv * 16 + 4 * g) * 8);
    u32x4 q0, q1;
#pragma nounroll
    for (int sp = 0; sp < SPIN_CAP; ++sp) {
      const unsigned sent = (unsigned)hr;
      const bool hok = (unsigned)(hr >> 32) == hch;
      hr = __hip_atomic_load(min_, RLX_AGENT);
      q0 = __builtin_bit_cast(u32x4, __builtin_amdgcn_raw_buffer_load_b128(rg, go, 0, 16));
      q1 = __builtin_bit_cast(u32x4, __builtin_amdgcn_raw_buffer_load_b128(rg, go + 16, 0, 16));
      if (__any((unsigned)hr != sent)) __hip_atomic_store(mout, (hr << 32) | hch, RLX_AGENT);
      const bool ok = hok && q0[1] != INVALID && q0[3] != INVALID && q1[1] != INVALID && q1[3] != INVALID;
      if (__all(ok)) break;
      __builtin_amdgcn_s_sleep(1);
      asm volatile("" ::: "memory");
    }
    const float cnt = c < 15 ? 128.f : (float)(L - 15 * 128);
    const f32x4 f0 = __builtin_bit_cast(f32x4, q0), f1 = __builtin_bit_cast(f32x4, q1);
    const float mu[4] = {f0[0], f0[2], f1[0], f1[2]};
    const float m2[4] = {f0[1], f0[3], f1[1], f1[3]};
#pragma unroll
    for (int k = 0; k < 4; ++k) {
      const float mean = row16_sum(mu[k] * cnt) * (1.0f / (float)L);
      const float d = mu[k] - mean;
      const float var = row16_sum(m2[k] + cnt * d * d) * (1.0f / (float)L);
      const float rstd = __builtin_amdgcn_rsqf(var + 1e-5f);
      sc[k] = rstd * (float)gav[k];
      sh[k] = (float)bev[k] - mean * sc[k];
    }
  }
  STAMP(4);
#pragma unroll
  for (int m = 0; m < 8; ++m) {
    bf16x4 o;
#pragma unroll
    for (int k = 0; k < 4; ++k) o[k] = (__bf16)gelu_tanh(v[m][k] * sc[k] + sh[k]);
    if (lrow + m < L) *(bf16x4*)(out + ((size_t)(b * L + lrow + m)) * D + nb + 4 * g) = o;
  }
  STAMP(5);
}

extern "C" void kernel_launch(void* const* d_in, const int* in_sizes, int n_in,
                              void* d_out, int out_size, void* d_ws, size_t ws_size,
                              hipStream_t stream) {
  const __bf16* x = (const __bf16*)d_in[0];
  const __bf16* w = (const __bf16*)d_in[1];
  const __bf16* bias = (const __bf16*)d_in[2];
  const __bf16* gamma = (const __bf16*)d_in[3];
  const __bf16* beta = (const __bf16*)d_in[4];
  unsigned long long* gran = (unsigned long long*)d_ws;
  unsigned long long* stamps = (unsigned long long*)((char*)d_ws + (size_t)32 * 1024 * 1024);
  conv_gn_gelu_kernel<<<256, 256, 0, stream>>>(x, w, bias, gamma, beta, (__bf16*)d_out, gran, stamps);
}
```

```cpp
#include <hip/hip_runtime.h>
#include <type_traits>

typedef __bf16 bf16x4 __attribute__((ext_vector_type(4)));
typedef __bf16 bf16x8 __attribute__((ext_vector_type(8)));
typedef float f32x4 __attribute__((ext_vector_type(4)));
typedef float f32x2 __attribute__((ext_vector_type(2)));
typedef unsigned u32x2 __attribute__((ext_vector_type(2)));
typedef unsigned u32x4 __attribute__((ext_vector_type(4)));
#define LDS_AS __attribute__((address_space(3)))
#define GAS __attribute__((address_space(1)))
#define DI __device__ __forceinline__

constexpr int NB = 2, T = 4000, D = 512, KT = 15, L = 1993;
constexpr int XIMG = 136 * 128;
constexpr int WREG = 2 * XIMG;
constexpr int LDS_BYTES = 4 * WREG;
constexpr int SPIN_CAP = 1 << 20;
constexpr unsigned INVALID = 0xFFFFFFFFu;
constexpr int GRAN_BYTES = 256 * 64 * 8;

#define RLX_AGENT __ATOMIC_RELAXED, __HIP_MEMORY_SCOPE_AGENT
#define WAIT_VM(n) asm volatile("s_waitcnt vmcnt(" #n ")" ::: "memory")
#define SCHED() __builtin_amdgcn_sched_barrier(0)
#define PF1 6
#define PF2 20
#define RING 5
#define XD 3
constexpr unsigned woff(int S) {
  return S < 16 ? (unsigned)((2 * (S & 7)) * (D * D * 2) + (S >> 3) * 32768)
                : (unsigned)((2 * ((S - 16) % 7) + 1) * (D * D * 2) + ((S - 16) / 7) * 32768);
}

template <unsigned long long MASK>
DI unsigned xsel(unsigned oth, unsigned own) {
  unsigned r;
  register unsigned long long m asm("vcc") = MASK;
  asm volatile("v_cndmask_b32_dpp %0, %1, %2, vcc quad_perm:[1,0,3,2] row_mask:0xf bank_mask:0xf" : "=v"(r) : "v"(oth), "v"(own), "s"(m));
  return r;
}
DI void mfma(f32x4& acc, const u32x4& a, const bf16x8& b) {
  asm volatile("v_mfma_f32_16x16x32_bf16 %0, %1, %2, %0" : "+a"(acc) : "v"(a), "v"(b));
}

DI float row16_sum(float v) {
  v += __builtin_bit_cast(float, __builtin_amdgcn_update_dpp(0, __builtin_bit_cast(int, v), 0xB1, 0xF, 0xF, true));
  v += __builtin_bit_cast(float, __builtin_amdgcn_update_dpp(0, __builtin_bit_cast(int, v), 0x4E, 0xF, 0xF, true));
  v += __builtin_bit_cast(float, __builtin_amdgcn_update_dpp(0, __builtin_bit_cast(int, v), 0x141, 0xF, 0xF, true));
  v += __builtin_bit_cast(float, __builtin_amdgcn_update_dpp(0, __builtin_bit_cast(int, v), 0x140, 0xF, 0xF, true));
  return v;
}
DI float gelu_tanh(float v) {
  const float t = v * (-2.3022081985f - 0.1029432397f * v * v);
  const float e = __builtin_amdgcn_exp2f(t);
  return v * __builtin_amdgcn_rcpf(1.0f + e);
}

#define STAMP(k) do {} while (0)

__global__ __launch_bounds__(256, 1)
void conv_gn_gelu_kernel(const __bf16* __restrict__ x, const __bf16* __restrict__ w, const __bf16* __restrict__ bias,
                         const __bf16* __restrict__ gamma, const __bf16* __restrict__ beta, __bf16* __restrict__ out,
                         unsigned long long* gran, unsigned long long* __restrict__ stamps) {
  __shared__ __attribute__((aligned(1024))) char smem[LDS_BYTES];
  LDS_AS char* const lds = (LDS_AS char*)smem;
  const int tid = threadIdx.x, lane = tid & 63;
  const int wv = __builtin_amdgcn_readfirstlane(tid >> 6);
  const int bid = blockIdx.x;
  const int xcd = bid & 7, idx = bid >> 3;
  const int mt = (xcd & 1) * 16 + (idx & 15), nt = (xcd >> 1) * 2 + (idx >> 4);
  const int b = mt >> 4, l0 = (mt & 15) * 128, n0 = nt * 64;
  const int c = lane & 15, g = lane >> 4;
  STAMP(0);

  const __amdgpu_buffer_rsrc_t rx = __builtin_amdgcn_make_buffer_rsrc((void*)x, 0, NB * T * D * 2, 0x00020000);
  const __amdgpu_buffer_rsrc_t rw = __builtin_amdgcn_make_buffer_rsrc((void*)w, 0, KT * D * D * 2, 0x00020000);
  LDS_AS char* const myl = lds + wv * WREG;

  const int grp = b * 8 + nt;
  GAS unsigned long long* const gown = (GAS unsigned long long*)gran + ((grp * 16 + (mt & 15)) * 64 + wv * 16);
  GAS unsigned long long* const mbase = (GAS unsigned long long*)gran + GRAN_BYTES / 8 + (grp * 4 + wv) * 256;
  GAS unsigned long long* const min_ = mbase + (mt & 15) * 16 + c;
  GAS unsigned long long* const mout = mbase + c * 16 + (mt & 15);

  const unsigned wo = (unsigned)((8 * g + 4 * (c & 1)) * 1024 + n0 * 2 + (c >> 1) * 16);
  u32x4 raw[RING][4];
  auto ldW = [&](int slot, int i, unsigned soff) {
    raw[slot][i] = __builtin_bit_cast(u32x4, __builtin_amdgcn_raw_buffer_load_b128(rw, wo + i * 1024, soff, 0));
  };
  unsigned wb = (unsigned)(wv * 65536);
#pragma unroll
  for (int i = 0; i < 4; ++i) ldW(0, i, wb + woff(0));
  SCHED();

  const unsigned xo0 = (unsigned)((b * T + 2 * l0 + 16 * (lane >> 3)) * (D * 2) + (((lane & 7) ^ (((lane >> 4) & 3) << 1)) * 16));
  const unsigned xo1 = (unsigned)((b * T + 2 * l0 + 256 + 2 * (lane >> 3)) * (D * 2) + (lane & 7) * 16);
  LDS_AS char* const xa0 = myl + lane * 16;
  LDS_AS char* const xa1 = myl + (17 * (lane >> 3) + 16) * 128 + (lane & 7) * 16;
  auto ldX = [&](int pc, unsigned so) -> u32x4 {
    return __builtin_bit_cast(u32x4, __builtin_amdgcn_raw_buffer_load_b128(rx, pc < 16 ? xo0 : xo1,
                                                                           so + (pc < 16 ? (pc >> 1) * 2048 + (pc & 1) * 131072 : 0), 0));
  };
  auto stX = [&](int p, int pc, const u32x4& v) {
    *(LDS_AS u32x4*)((pc < 16 ? xa0 : xa1) + p * XIMG + (pc < 16 ? (17 * (pc >> 1) + 8 * (pc & 1)) * 128 : 0)) = v;
  };
  u32x4 xs[17];
#pragma unroll
  for (int i = 0; i < 17; ++i) xs[i] = ldX(i, (unsigned)(wv * 128));
  SCHED();
  __hip_atomic_store(gown + c, ~0ull, RLX_AGENT);
  unsigned long long hr = __hip_atomic_load(min_, RLX_AGENT);
  SCHED();
#pragma unroll
  for (int s = 1; s < RING; ++s)
#pragma unroll
    for (int i = 0; i < 4; ++i) ldW(s, i, wb + woff(s));
  SCHED();

  const unsigned pfw = lane < 32 ? (unsigned)(((lane >> 2) * 2 * D + (mt & 15) * 4 + (lane & 3)) * (D * 2) + n0 * 2) : 0x80000000u;
  unsigned pf[1];
  pf[0] = __builtin_amdgcn_raw_buffer_load_b32(rw, pfw, wb, 0);
  SCHED();

  const int nb = n0 + wv * 16;
  __bf16 bv[4];
#pragma unroll
  for (int i = 0; i < 4; ++i) bv[i] = bias[nb + 4 * i + g];
  const bf16x4 gav = *(const bf16x4*)(gamma + nb + 4 * g), bev = *(const bf16x4*)(beta + nb + 4 * g);
  SCHED();

  unsigned fa[2][2];
#pragma unroll
  for (int hi = 0; hi < 2; ++hi) {
    const int v = c + hi;
    fa[0][hi] = (unsigned)(wv * WREG + v * 128 + ((g ^ (((v >> 1) & 3) << 1)) * 16));
    fa[1][hi] = fa[0][hi] ^ 64u;
  }
  auto ldF = [&](int pass, int q) -> bf16x8 {
    return *(LDS_AS bf16x8*)(lds + fa[pass & 1][q >> 3] + (pass >> 1) * XIMG + (q & 7) * 2176);
  };
  unsigned U[4][2], V[4][2];
  auto mkU = [&](int slot, int i) { U[i >> 1][i & 1] = xsel<0xAAAAAAAAAAAAAAAAull>(raw[slot][i >> 1][i & 1], raw[slot][i >> 1][2 + (i & 1)]); };
  auto mkV = [&](int slot, int i) { V[i >> 1][i & 1] = xsel<0x5555555555555555ull>(raw[slot][i >> 1][2 + (i & 1)], raw[slot][i >> 1][i & 1]); };
  auto mkB = [&](int e, int d) -> unsigned {
    const unsigned sel = (e & 1) ? 0x07060302u : 0x05040100u;
    return d < 2 ? __builtin_amdgcn_perm(V[2 * d + 1][e >> 1], V[2 * d][e >> 1], sel)
                 : __builtin_amdgcn_perm(U[2 * d - 3][e >> 1], U[2 * d - 4][e >> 1], sel);
  };

  f32x4 acc[8][4];
#pragma unroll
  for (int m = 0; m < 8; ++m)
#pragma unroll
    for (int e = 0; e < 4; ++e) acc[m][e] = f32x4{0.f, 0.f, 0.f, 0.f};

  bf16x8 F[2][15];
  u32x4 Bc[4], Bn[4];
#pragma unroll
  for (int i = 0; i < 8; ++i) mkU(0, i);
#pragma unroll
  for (int i = 0; i < 8; ++i) mkV(0, i);
#pragma unroll
  for (int e = 0; e < 4; ++e)
#pragma unroll
    for (int d = 0; d < 4; ++d) Bc[e][d] = mkB(e, d);
  SCHED();
#pragma unroll
  for (int i = 0; i < 4; ++i) ldW(0, i, wb + woff(RING));
  SCHED();
#pragma unroll
  for (int q = 0; q < 8; ++q) { stX(0, 2 * q, xs[2 * q]); stX(0, 2 * q + 1, xs[2 * q + 1]); F[0][q] = ldF(0, q); SCHED(); }
  stX(0, 16, xs[16]);
  STAMP(1);
  const unsigned hch = (unsigned)(hr >> 32) + 1u;
  __hip_atomic_store(mout, (hr << 32) | hch, RLX_AGENT);

  for (unsigned t = 0; t < 2; ++t) {
    const unsigned wn = t ? 0x40000000u : wb + 262144u;
    const unsigned xs1 = (unsigned)(wv * 128 + 1024) + t * 512u, xs0 = t ? 0x40000000u : (unsigned)(wv * 128 + 512);
    auto step = [&](auto SC) {
      constexpr int S = decltype(SC)::value;
      constexpr int PS = S < 8 ? 0 : S < 16 ? 1 : S < 23 ? 2 : 3;
      constexpr int J = S - (PS == 0 ? 0 : PS == 1 ? 8 : PS == 2 ? 16 : 23), NT = PS < 2 ? 8 : 7;
      constexpr int SL = (S + 1) % RING;
      constexpr int XL = S <= 8 ? 2 * S : (S >= 14 && S <= 22) ? 2 * (S - 14) : -1;
      constexpr int XW = (S >= XD && S <= 8 + XD) ? 2 * (S - XD) : (S >= 14 + XD && S <= 22 + XD) ? 2 * (S - 14 - XD) : -1;
      const unsigned so = (S + RING + 1 < 30) ? wb + woff(S + RING + 1) : wn + woff(S + RING + 1 - 30);
#pragma unroll
      for (int k = 0; k < 32; ++k) {
        const int e = k >> 3, m = k & 7;
        SCHED();
        mfma(acc[m][e], Bc[e], F[PS & 1][m + J]);
        SCHED();
        if (k < 8) mkU(SL, k);
        else if (k < 16) mkV(SL, k - 8);
        else Bn[(k - 16) >> 2][k & 3] = mkB((k - 16) >> 2, k & 3);
        if (k == 1 && J + 1 < NT) F[PS & 1][8 + J] = ldF(PS, 8 + J);
        if ((k == 3 || k == 5) && J >= NT - 4) F[(PS + 1) & 1][2 * (J - NT + 4) + (k == 5)] = ldF((PS + 1) & 3, 2 * (J - NT + 4) + (k == 5));
        if (k == 7 && XW >= 0) stX(S <= 8 + XD, XW, xs[XW]);
        if (k == 10 && XW >= 0 && XW < 16) stX(S <= 8 + XD, XW + 1, xs[XW + 1]);
        if (k == 13 && XL >= 0) xs[XL] = ldX(XL, S < 14 ? xs1 : xs0);
        if (k == 28 && XL >= 0 && XL < 16) xs[XL + 1] = ldX(XL + 1, S < 14 ? xs1 : xs0);
        if (k >= 16 && k < 26 && (k - 16) % 3 == 0) ldW(SL, (k - 16) / 3, so);
        if (k == 30 && (S == PF1 || S == PF2)) {
          asm volatile("" :: "v"(pf[0]));
          pf[0] = __builtin_amdgcn_raw_buffer_load_b32(rw, pfw, S == PF1 ? wb + D * D * 2 : wn, 0);
        }
      }
#pragma unroll
      for (int e = 0; e < 4; ++e) Bc[e] = Bn[e];
    };
#define ST(n) step(std::integral_constant<int, n>{})
    ST(0); ST(1); ST(2); ST(3); ST(4); ST(5); ST(6); ST(7); ST(8); ST(9); ST(10); ST(11); ST(12); ST(13); ST(14);
    ST(15); ST(16); ST(17); ST(18); ST(19); ST(20); ST(21); ST(22); ST(23); ST(24); ST(25); ST(26); ST(27); ST(28); ST(29);
    wb = wn;
  }
  asm volatile("s_nop 7\n\ts_nop 3" :: "v"(pf[0]) : "memory");
  STAMP(2);

  hr = __hip_atomic_load(min_, RLX_AGENT);
#pragma unroll
  for (int m = 0; m < 8; ++m)
#pragma unroll
    for (int e = 0; e < 4; ++e) *(LDS_AS f32x4*)(myl + ((m * 4 + e) * 64 + lane) * 16) = acc[m][e];
  __hip_atomic_store(mout, (hr << 32) | hch, RLX_AGENT);
  unsigned sent = (unsigned)hr;
  __syncthreads();
  hr = __hip_atomic_load(min_, RLX_AGENT);
  f32x4 v[8];
#pragma unroll
  for (int m = 0; m < 8; ++m) {
    const LDS_AS char* p = lds + ((m * 4 + g) * 64 + wv * 16 + c) * 16;
    const f32x4 p0 = *(const LDS_AS f32x4*)(p), p1 = *(const LDS_AS f32x4*)(p + WREG);
    const f32x4 p2 = *(const LDS_AS f32x4*)(p + 2 * WREG), p3 = *(const LDS_AS f32x4*)(p + 3 * WREG);
    v[m] = (p0 + p1) + (p2 + p3);
#pragma unroll
    for (int i = 0; i < 4; ++i) v[m][i] += (float)bv[i];
  }
  STAMP(3);
  const int lrow = l0 + 8 * c;
  {
    const float inv = 1.0f / (float)min(L - l0, 128);
    f32x4 s1 = {0.f, 0.f, 0.f, 0.f}, s2 = {0.f, 0.f, 0.f, 0.f}, mean;
#pragma unroll
    for (int m = 0; m < 8; ++m) if (lrow + m < L) s1 += v[m];
#pragma unroll
    for (int i = 0; i < 4; ++i) mean[i] = row16_sum(s1[i]) * inv;
#pragma unroll
    for (int m = 0; m < 8; ++m) if (lrow + m < L) { const f32x4 d = v[m] - mean; s2 += d * d; }
#pragma unroll
    for (int i = 0; i < 4; ++i) s2[i] = row16_sum(s2[i]);
    const float pm = c == 0 ? mean[0] : c == 1 ? mean[1] : c == 2 ? mean[2] : mean[3];
    const float ps = c == 0 ? s2[0] : c == 1 ? s2[1] : c == 2 ? s2[2] : s2[3];
    unsigned us = __builtin_bit_cast(unsigned, ps);
    if (us == INVALID) us = 0x7FC00000u;
    if (c < 4) __hip_atomic_store(gown + 4 * c + g, ((unsigned long long)us << 32) | __builtin_bit_cast(unsigned, pm), RLX_AGENT);
  }
  STAMP(7);
#pragma unroll
  for (int m = 0; m < 8; ++m) {
    u32x4 a = __builtin_bit_cast(u32x4, v[m]);
    auto r02 = __builtin_amdgcn_permlane32_swap(a[0], a[2], false, false);
    auto r13 = __builtin_amdgcn_permlane32_swap(a[1], a[3], false, false);
    auto r01 = __builtin_amdgcn_permlane16_swap(r02[0], r13[0], false, false);
    auto r23 = __builtin_amdgcn_permlane16_swap(r02[1], r13[1], false, false);
    v[m] = __builtin_bit_cast(f32x4, u32x4{r01[0], r01[1], r23[0], r23[1]});
  }
  float sc[4], sh[4];
  {
    const __amdgpu_buffer_rsrc_t rg = __builtin_amdgcn_make_buffer_rsrc((void*)gran, 0, GRAN_BYTES, 0x00020000);
    const unsigned go = (unsigned)(((grp * 16 + c) * 64 + wv * 16 + 4 * g) * 8);
    u32x4 q0, q1;
#pragma nounroll
    for (int sp = 0; sp < SPIN_CAP; ++sp) {
      if (__any((unsigned)hr != sent)) { __hip_atomic_store(mout, (hr << 32) | hch, RLX_AGENT); sent = (unsigned)hr; }
      const bool hok = (unsigned)(hr >> 32) == hch;
      hr = __hip_atomic_load(min_, RLX_AGENT);
      q0 = __builtin_bit_cast(u32x4, __builtin_amdgcn_raw_buffer_load_b128(rg, go, 0, 16));
      q1 = __builtin_bit_cast(u32x4, __builtin_amdgcn_raw_buffer_load_b128(rg, go + 16, 0, 16));
      const bool ok = hok && q0[1] != INVALID && q0[3] != INVALID && q1[1] != INVALID && q1[3] != INVALID;
      if (__all(ok)) break;
      __builtin_amdgcn_s_sleep(1);
      asm volatile("" ::: "memory");
    }
    const float cnt = c < 15 ? 128.f : (float)(L - 15 * 128);
    const f32x4 f0 = __builtin_bit_cast(f32x4, q0), f1 = __builtin_bit_cast(f32x4, q1);
    const float mu[4] = {f0[0], f0[2], f1[0], f1[2]};
    const float m2[4] = {f0[1], f0[3], f1[1], f1[3]};
#pragma unroll
    for (int k = 0; k < 4; ++k) {
      const float mean = row16_sum(mu[k] * cnt) * (1.0f / (float)L);
      const float d = mu[k] - mean;
      const float var = row16_sum(m2[k] + cnt * d * d) * (1.0f / (float)L);
      const float rstd = __builtin_amdgcn_rsqf(var + 1e-5f);
      sc[k] = rstd * (float)gav[k];
      sh[k] = (float)bev[k] - mean * sc[k];
    }
  }
  STAMP(4);
#pragma unroll
  for (int m = 0; m < 8; ++m) {
    bf16x4 o;
#pragma unroll
    for (int k = 0; k < 4; ++k) o[k] = (__bf16)gelu_tanh(v[m][k] * sc[k] + sh[k]);
    if (lrow + m < L) *(bf16x4*)(out + ((size_t)(b * L + lrow + m)) * D + nb + 4 * g) = o;
  }
  STAMP(5);
}

extern "C" void kernel_launch(void* const* d_in, const int* in_sizes, int n_in,
                              void* d_out, int out_size, void* d_ws, size_t ws_size,
                              hipStream_t stream) {
  const __bf16* x = (const __bf16*)d_in[0];
  const __bf16* w = (const __bf16*)d_in[1];
  const __bf16* bias = (const __bf16*)d_in[2];
  const __bf16* gamma = (const __bf16*)d_in[3];
  const __bf16* beta = (const __bf16*)d_in[4];
  unsigned long long* gran = (unsigned long long*)d_ws;
  unsigned long long* stamps = (unsigned long long*)((char*)d_ws + (size_t)32 * 1024 * 1024);
  conv_gn_gelu_kernel<<<256, 256, 0, stream>>>(x, w, bias, gamma, beta, (__bf16*)d_out, gran, stamps);
}
```

```cpp
#include <hip/hip_runtime.h>
#include <type_traits>

typedef __bf16 bf16x2 __attribute__((ext_vector_type(2)));
typedef __bf16 bf16x4 __attribute__((ext_vector_type(4)));
typedef __bf16 bf16x8 __attribute__((ext_vector_type(8)));
typedef float f32x4 __attribute__((ext_vector_type(4)));
typedef float f32x2 __attribute__((ext_vector_type(2)));
typedef unsigned u32x2 __attribute__((ext_vector_type(2)));
typedef unsigned u32x4 __attribute__((ext_vector_type(4)));
#define LDS_AS __attribute__((address_space(3)))
#define GAS __attribute__((address_space(1)))
#define DI __device__ __forceinline__

constexpr int NB = 2, T = 4000, D = 512, KT = 15, L = 1993;
constexpr int XIMG = 136 * 128;
constexpr int WREG = 2 * XIMG;
constexpr int LDS_BYTES = 4 * WREG;
constexpr int SPIN_CAP = 1 << 20;
constexpr unsigned INVALID = 0xFFFFFFFFu;
constexpr int GRAN_BYTES = 256 * 64 * 8;

#define RLX_AGENT __ATOMIC_RELAXED, __HIP_MEMORY_SCOPE_AGENT
#define MODD 0xFF00FF00FF00FF00ull
#define UREV 3
#define ALIGN 0
#define ALN
#define ALN2
#define SCHED() __builtin_amdgcn_sched_barrier(0)
#define PF1 6
#define PF2 20
#define RING 3
#define NPRE 12
#define NQ 3
#define HS 2
#define XD 3
constexpr unsigned woff(int S) {
  return S < 16 ? (unsigned)((2 * (S & 7)) * (D * D * 2) + (S >> 3) * 32768)
                : (unsigned)((2 * ((S - 16) % 7) + 1) * (D * D * 2) + ((S - 16) / 7) * 32768);
}
constexpr int xst(int im, int p) {
  const int a = im == 0 ? XD : im == 1 ? 16 : 30, z = im == 0 ? 15 : im == 1 ? 29 : 45;
  return a + (p * (z - a) + 8) / 16;
}
constexpr int xq(int G, int n) {
  for (int im = 0; im < 3; ++im)
    for (int p = 0; p < 17; ++p)
      if (xst(im, p) == G && n-- == 0) return im * 32 + p;
  return -1;
}
constexpr int wcnt(int G) {
  const int n = 4 * (G + RING + 1) + (G > 43 ? 2 * (G - 43) : 0);
  return n < 240 ? n : 240;
}

constexpr bool heavy(int G, int k) {
  const int wn = wcnt(G + 1) - wcnt(G);
  return (k == 7 && xq(G, 0) >= 0) || (k == 10 && xq(G, 1) >= 0) || (k == 13 && xq(G + XD, 0) >= 0) || (k == 28 && xq(G + XD, 1) >= 0) ||
         (wn <= 4 ? (k >= 16 && (k - 16) % 3 == 0 && (k - 16) / 3 < wn) : (k >= 10 && (k - 10) % 4 == 0 && (k - 10) / 4 < wn));
}
constexpr int vcum(int G, int k) { return k <= 0 ? 0 : heavy(G, k - 1) ? k - 1 : k; }

template <unsigned long long MASK, int X = 8>
DI unsigned xsel(unsigned oth, unsigned own) {
  unsigned r;
  register unsigned long long m asm("vcc") = MASK;
  if (X == 8) asm volatile(ALN2 "v_cndmask_b32_dpp %0, %1, %2, vcc row_ror:8 row_mask:0xf bank_mask:0xf" : "=v"(r) : "v"(oth), "v"(own), "s"(m));
  if (X == 1) asm volatile("v_cndmask_b32_dpp %0, %1, %2, vcc quad_perm:[1,0,3,2] row_mask:0xf bank_mask:0xf" : "=v"(r) : "v"(oth), "v"(own), "s"(m));
  if (X == 2) asm volatile("v_cndmask_b32_dpp %0, %1, %2, vcc quad_perm:[2,3,0,1] row_mask:0xf bank_mask:0xf" : "=v"(r) : "v"(oth), "v"(own), "s"(m));
  return r;
}
DI void mfma(f32x4& acc, const u32x4& a, const bf16x8& b) {
  asm volatile(ALN "v_mfma_f32_16x16x32_bf16 %0, %1, %2, %0" : "+a"(acc) : "v"(a), "v"(b));
}
DI void mfma0(f32x4& acc, const u32x4& a, const bf16x8& b) {
  asm volatile("v_mfma_f32_16x16x32_bf16 %0, %1, %2, 0" : "=a"(acc) : "v"(a), "v"(b));
}
template <int CTRL>
DI float dpp(float v) { return __builtin_bit_cast(float, __builtin_amdgcn_update_dpp(0, __builtin_bit_cast(int, v), CTRL, 0xF, 0xF, true)); }

DI float swadd32(float a, float b) {
  auto r = __builtin_amdgcn_permlane32_swap(__builtin_bit_cast(unsigned, a), __builtin_bit_cast(unsigned, b), false, false);
  return __builtin_bit_cast(float, (unsigned)r[0]) + __builtin_bit_cast(float, (unsigned)r[1]);
}
DI float swadd16(float a, float b) {
  auto r = __builtin_amdgcn_permlane16_swap(__builtin_bit_cast(unsigned, a), __builtin_bit_cast(unsigned, b), false, false);
  return __builtin_bit_cast(float, (unsigned)r[0]) + __builtin_bit_cast(float, (unsigned)r[1]);
}
DI f32x4 rows4(float x) {
  const unsigned u = __builtin_bit_cast(unsigned, x);
  auto h = __builtin_amdgcn_permlane32_swap(u, u, false, false);
  auto lo = __builtin_amdgcn_permlane16_swap(h[0], h[0], false, false);
  auto hi = __builtin_amdgcn_permlane16_swap(h[1], h[1], false, false);
  return __builtin_bit_cast(f32x4, u32x4{lo[0], lo[1], hi[0], hi[1]});
}

#define STAMP(k) do {} while (0)

__global__ __launch_bounds__(256, 1)
void conv_gn_gelu_kernel(const __bf16* __restrict__ x, const __bf16* __restrict__ w, const __bf16* __restrict__ bias,
                         const __bf16* __restrict__ gamma, const __bf16* __restrict__ beta, __bf16* __restrict__ out,
                         unsigned long long* gran, unsigned long long* __restrict__ stamps) {
  __shared__ __attribute__((aligned(1024))) char smem[LDS_BYTES];
  LDS_AS char* const lds = (LDS_AS char*)smem;
  const int tid = threadIdx.x, lane = tid & 63;
  const int wv = __builtin_amdgcn_readfirstlane(tid >> 6);
  const int bid = blockIdx.x;
  const int xcd = bid & 7, idx = bid >> 3;
  const int mt = (xcd & 1) * 16 + (idx & 15), nt = (xcd >> 1) * 2 + (idx >> 4);
  const int b = mt >> 4, l0 = (mt & 15) * 128, n0 = nt * 64;
  const int c = lane & 15, g = lane >> 4;
  STAMP(0);

  const __amdgpu_buffer_rsrc_t rx = __builtin_amdgcn_make_buffer_rsrc((void*)x, 0, NB * T * D * 2, 0x00020000);
  const __amdgpu_buffer_rsrc_t rw = __builtin_amdgcn_make_buffer_rsrc((void*)w, 0, KT * D * D * 2, 0x00020000);
  LDS_AS char* const myl = lds + wv * WREG;

  const int grp = b * 8 + nt;
  GAS unsigned long long* const gown = (GAS unsigned long long*)gran + ((grp * 16 + (mt & 15)) * 64 + wv * 16);
  GAS unsigned long long* const mbase = (GAS unsigned long long*)gran + GRAN_BYTES / 8 + (grp * 4 + wv) * 256;
  GAS unsigned long long* const min_ = mbase + (mt & 15) * 16 + c;
  GAS unsigned long long* const mout = mbase + c * 16 + (mt & 15);

  unsigned wo = (unsigned)((8 * g + 4 * (c >> 3)) * 1024 + n0 * 2 + (c & 7) * 16 + wv * 65536);
  u32x4 raw[60][4];
  auto ldW = [&](int n) {
    if (n == 120) wo += 262144;
    raw[n >> 2][n & 3] = __builtin_bit_cast(u32x4, __builtin_amdgcn_raw_buffer_load_b128(rw, wo + (n & 3) * 1024, woff((n >> 2) % 30), 0));
  };
#pragma unroll
  for (int i = 0; i < 4; ++i) ldW(i);
  SCHED();

  unsigned xo0 = (unsigned)((b * T + 2 * l0 + 16 * (lane >> 3)) * (D * 2) + (((lane & 7) ^ (((lane >> 4) & 3) << 1)) * 16) + wv * 128);
  unsigned xo1 = (unsigned)((b * T + 2 * l0 + 256 + 2 * (lane >> 3)) * (D * 2) + (lane & 7) * 16 + wv * 128);
  LDS_AS char* const xa0 = myl + lane * 16;
  LDS_AS char* const xa1 = myl + (17 * (lane >> 3) + 16) * 128 + (lane & 7) * 16;
  auto ldX = [&](int pc) -> u32x4 {
    return __builtin_bit_cast(u32x4, __builtin_amdgcn_raw_buffer_load_b128(rx, pc < 16 ? xo0 : xo1,
                                                                           pc < 16 ? (pc >> 1) * 2048 + (pc & 1) * 131072 : 0, 0));
  };
  auto stX = [&](int p, int pc, const u32x4& v) {
    *(LDS_AS u32x4*)((pc < 16 ? xa0 : xa1) + p * XIMG + (pc < 16 ? (17 * (pc >> 1) + 8 * (pc & 1)) * 128 : 0)) = v;
  };
  u32x4 xs[32];
  unsigned long long hr;
  auto ldS = [&](int n) {
    if (n < 16) xs[n & 15] = ldX(n);
    else if (n < 20) ldW(n - 12);
    else if (n == 20) xs[16] = ldX(16);
    else if (n < 21 + 4 * (RING - 1)) {
      ldW(n - 13);
      if (n == 20 + 4 * (HS - 1)) { __hip_atomic_store(gown + c, ~0ull, RLX_AGENT); hr = __hip_atomic_load(min_, RLX_AGENT); }
    }
  };
#pragma unroll
  for (int i = 0; i < NPRE; ++i) { ldS(i); SCHED(); }

  unsigned pfw = lane < 32 ? (unsigned)(((lane >> 2) * 2 * D + (mt & 15) * 4 + (lane & 3)) * (D * 2) + n0 * 2 + wv * 65536) : 0x80000000u;
  unsigned pf;

  unsigned fa[2][2];
#pragma unroll
  for (int hi = 0; hi < 2; ++hi) {
    const int v = c + hi;
    fa[0][hi] = (unsigned)(wv * WREG + v * 128 + ((g ^ (((v >> 1) & 3) << 1)) * 16));
    fa[1][hi] = fa[0][hi] ^ 64u;
  }
  auto ldF = [&](int pass, int q) -> bf16x8 {
    return *(LDS_AS bf16x8*)(lds + fa[pass & 1][q >> 3] + (pass >> 1) * XIMG + (q & 7) * 2176);
  };
  unsigned U[60][4][2], V[60][4][2];
  u32x4 B[60][4];
  auto mkU = [&](int s, int r, int j) { U[s][r][j] = xsel<MODD>(raw[s][r][j], raw[s][r][2 + j]); };
  auto mkV = [&](int s, int r, int j) { V[s][r][j] = xsel<~MODD>(raw[s][r][2 + j], raw[s][r][j]); };
  auto mkB = [&](int s, int e, int d) {
    const unsigned sel = (e & 1) ? 0x07060302u : 0x05040100u;
    B[s][e][d] = d < 2 ? __builtin_amdgcn_perm(V[s][2 * d + 1][e >> 1], V[s][2 * d][e >> 1], sel)
                       : __builtin_amdgcn_perm(U[s][2 * d - 3][e >> 1], U[s][2 * d - 4][e >> 1], sel);
  };

  f32x4 acc[8][4];
  bf16x8 F[2][15];
#pragma unroll
  for (int i = 0; i < 8; ++i) mkU(0, i >> 1, i & 1);
#pragma unroll
  for (int i = 0; i < 8; ++i) mkV(0, i >> 1, i & 1);
#pragma unroll
  for (int i = 0; i < 16; ++i) mkB(0, i >> 2, i & 3);
  SCHED();
#pragma unroll
  for (int q = 0; q <= 8; ++q) {
    if (q < 8) { stX(0, 2 * q, xs[2 * q]); stX(0, 2 * q + 1, xs[2 * q + 1]); F[0][q] = ldF(0, q); }
#pragma unroll
    for (int e = 0; e < 4; ++e) {
      SCHED();
      if (q > 0) mfma0(acc[q - 1][e], B[0][e], F[0][q - 1]);
      SCHED();
      if (e < NQ) ldS(NPRE + NQ * q + e);
    }
  }
  static_assert(NPRE + NQ * 9 >= 21 + 4 * (RING - 1), "");
  xo0 += 1024; xo1 += 1024;
  SCHED();
#pragma unroll
  for (int i = 0; i < 8; ++i) mkU(1, i >> 1, i & 1);
#pragma unroll
  for (int i = 0; i < 8; ++i) mkV(1, i >> 1, i & 1);
  stX(0, 16, xs[16]);
  F[0][8] = ldF(0, 8);
#pragma unroll
  for (int i = 0; i < 16; ++i) mkB(1, i >> 2, i & 3);
  SCHED();
#pragma unroll
  for (int i = 0; i < 4; ++i) ldW(wcnt(0) + i);
  xs[0] = ldX(0);
  if (xq(XD, 1) >= 0) xs[1] = ldX(1);
  pf = __builtin_amdgcn_raw_buffer_load_b32(rw, pfw, 0, 0);
  SCHED();
  STAMP(1);
  unsigned hch, sent;

  const int cb = n0 + 32 * (wv & 1) + 4 * (wv >> 1);
  __bf16 bi1, bi2, ga1, be1;
  unsigned ln;
  auto step = [&](auto GC) {
    constexpr int G = decltype(GC)::value, S = G % 30;
    constexpr int PS = S < 8 ? 0 : S < 16 ? 1 : S < 23 ? 2 : 3;
    constexpr int J = S - (PS == 0 ? 0 : PS == 1 ? 8 : PS == 2 ? 16 : 23), NT = PS < 2 ? 8 : 7;
    constexpr int W0 = xq(G, 0), W1 = xq(G, 1), L0 = xq(G + XD, 0), L1 = xq(G + XD, 1);
    constexpr int WA = wcnt(G), WN = wcnt(G + 1) - WA;
    static_assert(xq(G, 2) < 0, "");
#pragma unroll
    for (int k = 0; k < 32; ++k) {
      const int e = k >> 3, m = k & 7;
      SCHED();
      mfma(acc[m][e], B[G][e], F[PS & 1][m + J]);
      SCHED();
#pragma unroll
      for (int u = vcum(G, k); u < vcum(G, k + 1); ++u) {
        if (u < 8) mkU(G + 1, UREV ^ (u >> 1), u & 1);
        else if (u < 16) mkV(G + 1, (u - 8) >> 1, u & 1);
        else mkB(G + 1, (u - 16) >> 2, u & 3);
      }
      if (G >= 53) {
        if (k < 8) { if (k & 1) mkU(G + 4, k >> 1, 0); }
        else if (k < 16) { if (k & 1) mkV(G + 4, (k - 8) >> 1, 0); }
        else if (k & 1) mkB(G + 4, (k - 16) >> 3, (k >> 1) & 3);
      }
      if (k == 1 && J + 1 < NT) F[PS & 1][(8 + J) % 15] = ldF(PS, 8 + J);
      if (G == 55 && (k == 3 || k == 5 || k == 7)) F[1][10 + (k >> 1)] = ldF(3, 10 + (k >> 1));
      if (G < 53 && (k == 3 || k == 5) && J >= NT - 4) F[(PS + 1) & 1][(2 * (J - NT + 4) + (k == 5)) & 7] = ldF((PS + 1) & 3, 2 * (J - NT + 4) + (k == 5));
      if (k == 7 && W0 >= 0) stX((W0 >> 5) != 1, W0 & 31, xs[W0 & 31]);
      if (k == 10 && W1 >= 0) stX((W1 >> 5) != 1, W1 & 31, xs[W1 & 31]);
      if (k == 12 && L0 == 32) { xo0 -= 512; xo1 -= 512; pfw += 262144; }
      if (k == 12 && L0 == 64) { xo0 += 1024; xo1 += 1024; }
      if (k == 13 && L0 >= 0) xs[L0 & 31] = ldX(L0 & 31);
      if (k == 28 && L1 >= 0) xs[L1 & 31] = ldX(L1 & 31);
      if (WN <= 4) { if (k >= 16 && (k - 16) % 3 == 0 && (k - 16) / 3 < WN) ldW(WA + (k - 16) / 3); }
      else if (k >= 10 && (k - 10) % 4 == 0 && (k - 10) / 4 < WN) ldW(WA + (k - 10) / 4);
      if (k == 30 && (S == PF1 || G == PF2)) {
        asm volatile("" :: "v"(pf));
        pf = __builtin_amdgcn_raw_buffer_load_b32(rw, pfw, S == PF1 ? D * D * 2 : 0, 0);
      }
      if (G == HS && k == 29) {
        hch = (unsigned)(hr >> 32) + 1u;
        __hip_atomic_store(mout, (hr << 32) | hch, RLX_AGENT);
      }
      if (G == 51 && k == 30) hr = __hip_atomic_load(min_, RLX_AGENT);
      if (G == 54 && k == 30) {
        asm volatile("" :: "v"(pf));
        __hip_atomic_store(mout, (hr << 32) | hch, RLX_AGENT);
        sent = (unsigned)hr;
      }
      if (G == 52 && k == 16) {
        static_assert(wcnt(52) == 240, "");
        asm volatile("v_mbcnt_lo_u32_b32 %0, -1, 0\n\tv_mbcnt_hi_u32_b32 %0, -1, %0" : "=v"(ln));
        bi1 = bias[cb + 8 * (ln >> 4) + (ln & 3)]; bi2 = bias[cb + 8 * (ln & 3) + (ln >> 4)]; ga1 = gamma[cb + 8 * (ln & 3) + (ln >> 4)]; be1 = beta[cb + 8 * (ln & 3) + (ln >> 4)];
      }
    }
  };
#define ST(n) step(std::integral_constant<int, n>{})
#define ST10(n) ST(n); ST(n + 1); ST(n + 2); ST(n + 3); ST(n + 4); ST(n + 5); ST(n + 6); ST(n + 7); ST(n + 8); ST(n + 9)
  ST(1); ST(2); ST(3); ST(4); ST(5); ST(6); ST(7); ST(8); ST(9);
  ST10(10); ST10(20); ST10(30); ST10(40); ST(50); ST(51); ST(52); ST(53); ST(54); ST(55);
  unsigned la[4], gpub, go;
  const __amdgpu_buffer_rsrc_t rg = __builtin_amdgcn_make_buffer_rsrc((void*)gran, 0, GRAN_BYTES, 0x00020000);
  auto stA = [&](int a) { *(LDS_AS f32x4*)(xa0 + ((a & 7) * 4 + (a >> 3)) * 1088) = acc[a & 7][a >> 3]; };
#define TI 1
#pragma unroll
  for (int n4 = 0; n4 < 32; ++n4)
#pragma unroll
  for (int n = 4 * n4; n < 4 * n4 + 4; ++n) {
    const int a = (n / (4 * TI)) * TI + n % TI, j = (n / TI) & 3;
    const int e = a >> 3, m = a & 7, k = n & 31;
    SCHED();
    mfma(acc[m][e], B[56 + j][e], F[1][m + 3 + j]);
    SCHED();
    if (e == 0 && k < 12) mkU(57 + (k >> 2), k & 3, 1);
    if (e == 0 && k >= 12 && k < 24) mkV(57 + ((k - 12) >> 2), k & 3, 1);
    if (e == 1 && k < 24) mkB(57 + (k % 12 >> 2), 2 + k / 12, k & 3);
    if ((n & 3) == 3 && (n >> 2) >= TI + 1) stA((n >> 2) - TI - 1);
    if (n == 88) hr = __hip_atomic_load(min_, RLX_AGENT);
    if (n == 72) {
      go = (unsigned)(((grp * 16 + (ln & 12)) * 64 + wv * 16 + 4 * (ln & 3) + (ln >> 4)) * 8);
      la[0] = (ln & 3) * 1088 + wv * 256; la[1] = la[0] + 2 * WREG; la[2] = la[0] + (ln >> 2) * 16; la[3] = la[2] + 2 * WREG;
      gpub = (ln & 12) ? 0x80000000u : (unsigned)(((grp * 16 + (mt & 15)) * 64 + wv * 16) * 8) + ((ln >> 2) + (ln & 3)) * 8;
    }
  }
  STAMP(2);
  asm volatile("s_nop 7" ::: "memory");
  SCHED();
#pragma unroll
  for (int a = 31 - TI; a < 32; ++a) stA(a);
  asm volatile("s_waitcnt lgkmcnt(0)\n\ts_barrier" : "+v"(la[0]), "+v"(la[1]), "+v"(la[2]), "+v"(la[3]), "+v"(gpub), "+v"(go) :: "memory");

  f32x4 v[8], piv, d0, s1, s2;
  float pv;
  bool hok;
  {
    f32x4 p[8][4], pz[4];
#pragma unroll
    for (int ww = 0; ww < 4; ++ww) pz[ww] = *(const LDS_AS f32x4*)(lds + la[ww >> 1] + (ww & 1) * WREG);
#pragma unroll
    for (int m = 0; m < 3; ++m)
#pragma unroll
      for (int ww = 0; ww < 4; ++ww) p[m][ww] = *(const LDS_AS f32x4*)(lds + la[2 + (ww >> 1)] + (ww & 1) * WREG + m * 4352);
    SCHED();
    piv = (pz[0] + pz[1]) + (pz[2] + pz[3]);
#pragma unroll
    for (int m = 0; m < 8; ++m) {
      if (m + 3 < 8) {
#pragma unroll
        for (int ww = 0; ww < 4; ++ww) p[(m + 3) & 7][ww] = *(const LDS_AS f32x4*)(lds + la[2 + (ww >> 1)] + (ww & 1) * WREG + (m + 3) * 4352);
      }
      SCHED();
      v[m] = (p[m][0] + p[m][1]) + (p[m][2] + p[m][3]);
      const f32x4 d = v[m] - piv;
      if (m == 0) {
        d0 = d;
        const float p01 = (ln & 16) ? piv[1] : piv[0], p23 = (ln & 16) ? piv[3] : piv[2];
        pv = ((ln & 32) ? p23 : p01) + (float)bi1;
        asm volatile("" : "+v"(pv));
        hok = __all((unsigned)(hr >> 32) == hch && (unsigned)hr == sent);
      }
      else { s1 = m == 1 ? d : s1 + d; s2 = m == 1 ? d * d : s2 + d * d; }
      SCHED();
    }
  }
  STAMP(3);
  const bool last = (mt & 15) == 15;
  float mown;
  {
    if (last) {
      const f32x4 z = {0.f, 0.f, 0.f, 0.f};
      asm volatile("");
      if (ln >= 40) d0 = z;
      if (ln >= 36) { s1 = z; s2 = z; }
    }
    s1 += d0; s2 += d0 * d0;
    float a1 = swadd16(swadd32(s1[0], s1[2]), swadd32(s1[1], s1[3]));
    float a2 = swadd16(swadd32(s2[0], s2[2]), swadd32(s2[1], s2[3]));
    a1 += dpp<0x124>(a1); a2 += dpp<0x124>(a2);
    a1 += dpp<0x128>(a1); a2 += dpp<0x128>(a2);
    const float dm = a1 * (last ? 1.0f / (float)(L - 15 * 128) : 1.0f / 128.0f);
    mown = pv + dm;
    unsigned us = __builtin_bit_cast(unsigned, a2 - a1 * dm);
    if (us == INVALID) us = 0x7FC00000u;
    __builtin_amdgcn_raw_buffer_store_b64(u32x2{__builtin_bit_cast(unsigned, mown), us}, rg, gpub, 0, 16);
  }
  STAMP(7);
  if (!hok) {
#pragma nounroll
    for (int sp = 0; sp < SPIN_CAP; ++sp) {
      if (__any((unsigned)hr != sent)) { __hip_atomic_store(mout, (hr << 32) | hch, RLX_AGENT); sent = (unsigned)hr; }
      if (__all((unsigned)(hr >> 32) == hch)) break;
      __builtin_amdgcn_s_sleep(1);
      hr = __hip_atomic_load(min_, RLX_AGENT);
    }
    asm volatile("" :: "v"(hr));
  }
    asm volatile("" ::: "memory");
    u32x2 q[4];
    auto poll = [&](u32x2* r) {
#pragma unroll
      for (int j = 0; j < 4; ++j) r[j] = __builtin_bit_cast(u32x2, __builtin_amdgcn_raw_buffer_load_b64(rg, go + j * 512, 0, 16));
    };
    auto bad = [&](const u32x2* r) { return __any(max(max(r[0][1], r[1][1]), max(r[2][1], r[3][1])) == INVALID); };
  poll(q);
  {
    u32x4 a[8], t[8];
#pragma unroll
    for (int m = 0; m < 8; ++m) { a[m] = __builtin_bit_cast(u32x4, v[m]); t[m][0] = xsel<0x5555555555555555ull, 1>(a[m][1], a[m][0]); t[m][2] = xsel<0x5555555555555555ull, 1>(a[m][3], a[m][2]); }
#pragma unroll
    for (int m = 0; m < 8; ++m) { t[m][1] = xsel<0xAAAAAAAAAAAAAAAAull, 1>(a[m][0], a[m][1]); t[m][3] = xsel<0xAAAAAAAAAAAAAAAAull, 1>(a[m][2], a[m][3]); }
#pragma unroll
    for (int m = 0; m < 8; ++m) { a[m][0] = xsel<0x3333333333333333ull, 2>(t[m][2], t[m][0]); a[m][1] = xsel<0x3333333333333333ull, 2>(t[m][3], t[m][1]); }
#pragma unroll
    for (int m = 0; m < 8; ++m) { a[m][2] = xsel<0xCCCCCCCCCCCCCCCCull, 2>(t[m][0], t[m][2]); a[m][3] = xsel<0xCCCCCCCCCCCCCCCCull, 2>(t[m][1], t[m][3]); }
#pragma unroll
    for (int m = 0; m < 8; ++m) v[m] = __builtin_bit_cast(f32x4, a[m]);
  }
  const float pm = __builtin_bit_cast(float, __builtin_amdgcn_ds_bpermute(((ln & 3) * 16 + (ln >> 4)) * 4, __builtin_bit_cast(int, mown)));
  f32x4 sc, sh;
  {
    float gaf = (float)ga1, bef = (float)be1, pmb = pm - (float)bi2, n3 = (ln & 12) != 12 ? 128.f : (float)(L - 15 * 128);
    asm volatile("" : "+v"(gaf), "+v"(bef), "+v"(pmb), "+v"(n3));
#pragma nounroll
    for (int sp = 0; sp < SPIN_CAP; ++sp) {
      if (!bad(q)) break;
      __builtin_amdgcn_s_sleep(1);
      asm volatile("" ::: "memory");
      poll(q);
    }
    float d[4], m2[4];
#pragma unroll
    for (int j = 0; j < 4; ++j) { const f32x2 f = __builtin_bit_cast(f32x2, q[j]); d[j] = f[0] - pm; m2[j] = f[1]; }
    const float e3 = n3 * d[3];
    float a1 = 128.f * ((d[0] + d[1]) + d[2]) + e3;
    float a2 = 128.f * (d[0] * d[0] + d[1] * d[1] + d[2] * d[2]) + (e3 * d[3] + ((m2[0] + m2[1]) + (m2[2] + m2[3])));
    a1 += dpp<0x124>(a1); a2 += dpp<0x124>(a2);
    a1 += dpp<0x128>(a1); a2 += dpp<0x128>(a2);
    const float dm = a1 * (1.0f / (float)L);
    const float var = a2 * (1.0f / (float)L) - dm * dm;
    const float scl = __builtin_amdgcn_rsqf(var + 1e-5f) * gaf;
    const float shf = bef - (pmb + dm) * scl;
    sc = rows4(scl); sh = rows4(shf);
  }
  STAMP(4);
  const __amdgpu_buffer_rsrc_t ro = __builtin_amdgcn_make_buffer_rsrc((void*)(out + (size_t)b * L * D), 0, L * D * 2, 0x00020000);
  const unsigned oo = (unsigned)((l0 + 8 * (ln >> 2)) * (D * 2) + cb * 2 + (ln & 3) * 16);
#pragma unroll
  for (int m = 0; m < 8; ++m) {
    const f32x4 u = v[m] * sc + sh;
    const f32x4 t = u * (u * u * -0.1029432397f - 2.3022081985f);
    f32x4 r;
#pragma unroll
    for (int k = 0; k < 4; ++k) r[k] = __builtin_amdgcn_exp2f(t[k]);
    r += 1.0f;
#pragma unroll
    for (int k = 0; k < 4; ++k) r[k] = __builtin_amdgcn_rcpf(r[k]);
    r *= u;
    __builtin_amdgcn_raw_buffer_store_b64(__builtin_bit_cast(u32x2, bf16x4{(__bf16)r[0], (__bf16)r[1], (__bf16)r[2], (__bf16)r[3]}), ro, oo + m * 1024, 0, 0);
  }
  STAMP(5);
}

extern "C" void kernel_launch(void* const* d_in, const int* in_sizes, int n_in,
                              void* d_out, int out_size, void* d_ws, size_t ws_size,
                              hipStream_t stream) {
  const __bf16* x = (const __bf16*)d_in[0];
  const __bf16* w = (const __bf16*)d_in[1];
  const __bf16* bias = (const __bf16*)d_in[2];
  const __bf16* gamma = (const __bf16*)d_in[3];
  const __bf16* beta = (const __bf16*)d_in[4];
  unsigned long long* gran = (unsigned long long*)d_ws;
  unsigned long long* stamps = (unsigned long long*)((char*)d_ws + (size_t)32 * 1024 * 1024);
  conv_gn_gelu_kernel<<<256, 256, 0, stream>>>(x, w, bias, gamma, beta, (__bf16*)d_out, gran, stamps);
}
```

```cpp
#include <hip/hip_runtime.h>
#include <type_traits>

typedef __bf16 bf16x2 __attribute__((ext_vector_type(2)));
typedef __bf16 bf16x4 __attribute__((ext_vector_type(4)));
typedef __bf16 bf16x8 __attribute__((ext_vector_type(8)));
typedef float f32x4 __attribute__((ext_vector_type(4)));
typedef float f32x2 __attribute__((ext_vector_type(2)));
typedef unsigned u32x2 __attribute__((ext_vector_type(2)));
typedef unsigned u32x4 __attribute__((ext_vector_type(4)));
#define LDS_AS __attribute__((address_space(3)))
#define GAS __attribute__((address_space(1)))
#define DI __device__ __forceinline__

constexpr int NB = 2, T = 4000, D = 512, KT = 15, L = 1993;
constexpr int XIMG = 136 * 128;
constexpr int WREG = 2 * XIMG;
constexpr int LDS_BYTES = 4 * WREG;
constexpr int SPIN_CAP = 1 << 20;
constexpr unsigned INVALID = 0xFFFFFFFFu;
constexpr int GRAN_BYTES = 256 * 64 * 8;

#define RLX_AGENT __ATOMIC_RELAXED, __HIP_MEMORY_SCOPE_AGENT
#define MODD 0xFF00FF00FF00FF00ull
#define UREV 3
#define ALIGN 0
#define ALN
#define ALN2
#define SCHED() __builtin_amdgcn_sched_barrier(0)
#define PF1 6
#define PF2 20
#define PFM 0
#define PFD 7
#define RING 3
#define NPRE 8
#define NQ 3
#define HS 3
#define XD 3
constexpr unsigned woff(int S) {
  return S < 16 ? (unsigned)((2 * (S & 7)) * (D * D * 2) + (S >> 3) * 32768)
                : (unsigned)((2 * ((S - 16) % 7) + 1) * (D * D * 2) + ((S - 16) / 7) * 32768);
}
constexpr int pasof(int G) { return G % 30 < 8 ? 0 : G % 30 < 16 ? 1 : G % 30 < 23 ? 2 : 3; }
constexpr int posof(int G) { return G % 30 - (pasof(G) == 0 ? 0 : pasof(G) == 1 ? 8 : pasof(G) == 2 ? 16 : 23); }
#define WX 0
#define RS 1
#define CO 4
#define CPF 1
#define RS0 1
#define QW 1
constexpr bool quiet(int G) { return (G > PF1 && G <= PF1 + QW) || (G > PF2 && G <= PF2 + QW) || (G > PF1 + 30 && G <= PF1 + 30 + QW); }
constexpr int xst(int im, int p) {
  const int a = im == 0 ? XD : im == 1 ? 16 : 30, z = im == 0 ? 15 : im == 1 ? 29 : 45;
  int n = 0;
  for (int g = a; g <= z; ++g) n += !quiet(g - XD);
  int i = (p * (n - 1) + 8) / 16;
  for (int g = a; g <= z; ++g)
    if (!quiet(g - XD) && i-- == 0) return g;
  return -1;
}
constexpr int xq(int G, int n) {
  for (int im = 0; im < 3; ++im)
    for (int p = 0; p < 17; ++p)
      if (xst(im, p) == G && n-- == 0) return im * 32 + p;
  return -1;
}
constexpr int wcnt(int G) {
  int n = 4 * (G + RING + 1) + (G >= 2 ? WX : 0) + (G > 43 ? 2 * (G - 43) : 0);
  for (int i = 0; i < 3; ++i) {
    const int d = G - 1 - (i == 0 ? PF1 : i == 1 ? PF2 : PF1 + 30);
    const int rs = i == 0 ? RS0 : RS;
    n += d <= 0 ? (4 * QW + rs * d > 0 ? 4 * QW + rs * d : 0) : (4 * QW - 4 * d > 0 ? 4 * QW - 4 * d : 0);
  }
  return n < 240 ? n : 240;
}

#define PLAN 2
constexpr int KF = 0, KS0 = 6, KS1 = 10, KL0 = 12, KL1 = 28, KW = 14, KWD = 4, KW6 = 30, KW7 = 8;
constexpr unsigned wmask(int G) {
  int wn = wcnt(G + 1) - wcnt(G);
  const int pos[8] = {KW, KW + KWD, KW + 2 * KWD, KW + 3 * KWD, xq(G + XD, 0) < 0 ? KL0 : -1, xq(G + XD, 1) < 0 ? KL1 : -1, KW6, KW7};
  unsigned m = 0;
  for (int i = 0; i < 8; ++i)
    if (pos[i] >= 0 && wn > 0) { m |= 1u << pos[i]; --wn; }
  return m;
}
constexpr bool heavy(int G, int k) {
  return (k == KS0 && xq(G, 0) >= 0) || (k == KS1 && xq(G, 1) >= 0) || (k == KL0 && xq(G + XD, 0) >= 0) || (k == KL1 && xq(G + XD, 1) >= 0) || ((wmask(G) >> k) & 1);
}
constexpr unsigned hmask(int G) { unsigned m = 0; for (int k = 0; k < 31; ++k) m |= heavy(G, k) ? 2u << k : 0u; return m; }
constexpr int vcum(unsigned hm, int k) { return k & ~1; }

template <unsigned long long MASK, int X = 8>
DI unsigned xsel(unsigned oth, unsigned own) {
  unsigned r;
  register unsigned long long m asm("vcc") = MASK;
  if (X == 8) asm volatile(ALN2 "v_cndmask_b32_dpp %0, %1, %2, vcc row_ror:8 row_mask:0xf bank_mask:0xf" : "=v"(r) : "v"(oth), "v"(own), "s"(m));
  if (X == 1) asm volatile("v_cndmask_b32_dpp %0, %1, %2, vcc quad_perm:[1,0,3,2] row_mask:0xf bank_mask:0xf" : "=v"(r) : "v"(oth), "v"(own), "s"(m));
  if (X == 2) asm volatile("v_cndmask_b32_dpp %0, %1, %2, vcc quad_perm:[2,3,0,1] row_mask:0xf bank_mask:0xf" : "=v"(r) : "v"(oth), "v"(own), "s"(m));
  return r;
}
DI void mfma(f32x4& acc, const u32x4& a, const bf16x8& b) {
  asm volatile(ALN "v_mfma_f32_16x16x32_bf16 %0, %1, %2, %0" : "+a"(acc) : "v"(a), "v"(b));
}
DI void mfma0(f32x4& acc, const u32x4& a, const bf16x8& b) {
  asm volatile("v_mfma_f32_16x16x32_bf16 %0, %1, %2, 0" : "=a"(acc) : "v"(a), "v"(b));
}
template <int CTRL>
DI float dpp(float v) { return __builtin_bit_cast(float, __builtin_amdgcn_update_dpp(0, __builtin_bit_cast(int, v), CTRL, 0xF, 0xF, true)); }

DI float swadd32(float a, float b) {
  auto r = __builtin_amdgcn_permlane32_swap(__builtin_bit_cast(unsigned, a), __builtin_bit_cast(unsigned, b), false, false);
  return __builtin_bit_cast(float, (unsigned)r[0]) + __builtin_bit_cast(float, (unsigned)r[1]);
}
DI float swadd16(float a, float b) {
  auto r = __builtin_amdgcn_permlane16_swap(__builtin_bit_cast(unsigned, a), __builtin_bit_cast(unsigned, b), false, false);
  return __builtin_bit_cast(float, (unsigned)r[0]) + __builtin_bit_cast(float, (unsigned)r[1]);
}
DI f32x4 rows4(float x) {
  const unsigned u = __builtin_bit_cast(unsigned, x);
  auto h = __builtin_amdgcn_permlane32_swap(u, u, false, false);
  auto lo = __builtin_amdgcn_permlane16_swap(h[0], h[0], false, false);
  auto hi = __builtin_amdgcn_permlane16_swap(h[1], h[1], false, false);
  return __builtin_bit_cast(f32x4, u32x4{lo[0], lo[1], hi[0], hi[1]});
}

#define STAMP(k) do {} while (0)

__global__ __launch_bounds__(256, 1)
void conv_gn_gelu_kernel(const __bf16* __restrict__ x, const __bf16* __restrict__ w, const __bf16* __restrict__ bias,
                         const __bf16* __restrict__ gamma, const __bf16* __restrict__ beta, __bf16* __restrict__ out,
                         unsigned long long* gran, unsigned long long* __restrict__ stamps) {
  __shared__ __attribute__((aligned(1024))) char smem[LDS_BYTES];
  LDS_AS char* const lds = (LDS_AS char*)smem;
  const int tid = threadIdx.x, lane = tid & 63;
  const int wv = __builtin_amdgcn_readfirstlane(tid >> 6);
  const int bid = blockIdx.x;
  const int xcd = bid & 7, idx = bid >> 3;
  const int mt = (xcd & 1) * 16 + (idx & 15), nt = (xcd >> 1) * 2 + (idx >> 4);
  const int b = mt >> 4, l0 = (mt & 15) * 128, n0 = nt * 64;
  const int c = lane & 15, g = lane >> 4;
  STAMP(0);

  const __amdgpu_buffer_rsrc_t rx = __builtin_amdgcn_make_buffer_rsrc((void*)(x + (size_t)b * T * D), 0, T * D * 2, 0x00020000);
  const __amdgpu_buffer_rsrc_t rw = __builtin_amdgcn_make_buffer_rsrc((void*)w, 0, KT * D * D * 2, 0x00020000);
  LDS_AS char* const myl = lds + wv * WREG;

  const int grp = b * 8 + nt;
  GAS unsigned long long* const gown = (GAS unsigned long long*)gran + ((grp * 16 + (mt & 15)) * 64 + wv * 16);
  GAS unsigned long long* const mbase = (GAS unsigned long long*)gran + GRAN_BYTES / 8 + (grp * 4 + wv) * 256;
  GAS unsigned long long* const min_ = mbase + (mt & 15) * 16 + c;
  GAS unsigned long long* const mout = mbase + c * 16 + (mt & 15);

  unsigned wo = (unsigned)((8 * g + 4 * (c >> 3)) * 1024 + n0 * 2 + (c & 7) * 16 + (wv + CO) * 65536);
  u32x4 raw[60][4];
  auto ldW = [&](int n) {
    if (n == 120) wo ^= 262144;
    raw[n >> 2][n & 3] = __builtin_bit_cast(u32x4, __builtin_amdgcn_raw_buffer_load_b128(rw, wo + (n & 3) * 1024, woff((n >> 2) % 30), 0));
  };
#pragma unroll
  for (int i = 0; i < 4; ++i) ldW(i);
  SCHED();

  unsigned xo0 = (unsigned)((2 * l0 + 16 * (lane >> 3)) * (D * 2) + (((lane & 7) ^ (((lane >> 4) & 3) << 1)) * 16) + (wv + CO) * 128);
  unsigned xo1 = (unsigned)((2 * l0 + 256 + 2 * (lane >> 3)) * (D * 2) + (lane & 7) * 16 + (wv + CO) * 128);
  LDS_AS char* const xa0 = myl + lane * 16;
  LDS_AS char* const xa1 = myl + (17 * (lane >> 3) + 16) * 128 + (lane & 7) * 16;
  auto ldX = [&](int pc) -> u32x4 {
    return __builtin_bit_cast(u32x4, __builtin_amdgcn_raw_buffer_load_b128(rx, pc < 16 ? xo0 : xo1,
                                                                           pc < 16 ? (pc >> 1) * 2048 + (pc & 1) * 131072 : 0, 0));
  };
  auto stX = [&](int p, int pc, const u32x4& v) {
    *(LDS_AS u32x4*)((pc < 16 ? xa0 : xa1) + p * XIMG + (pc < 16 ? (17 * (pc >> 1) + 8 * (pc & 1)) * 128 : 0)) = v;
  };
  u32x4 xs[32];
  unsigned long long hr;
  auto ldS = [&](int n) {
    if (n < 16) xs[n & 15] = ldX(n);
    else if (n < 20) ldW(n - 12);
    else if (n == 20) xs[16] = ldX(16);
    else if (n < 21 + 4 * (RING - 1)) {
      ldW(n - 13);
      if (n == 20 + 4 * (HS - 1)) { __hip_atomic_store(gown + c, ~0ull, RLX_AGENT); hr = __hip_atomic_load(min_, RLX_AGENT); }
    }
  };
#pragma unroll
  for (int i = 0; i < NPRE; ++i) { ldS(i); SCHED(); }

  unsigned pfw = lane < 32 ? (unsigned)(((lane >> 2) * 2 * D + (mt & 15) * 4 + (lane & 3)) * (D * 2) + n0 * 2 + (wv + CO) * 65536) : 0x80000000u;
  unsigned pf = 0;

  unsigned fa[2][2];
#pragma unroll
  for (int hi = 0; hi < 2; ++hi) {
    const int v = c + hi;
    fa[0][hi] = (unsigned)(wv * WREG + v * 128 + ((g ^ (((v >> 1) & 3) << 1)) * 16));
    fa[1][hi] = fa[0][hi] ^ 64u;
  }
  auto ldF = [&](int pass, int q) -> bf16x8 {
    return *(LDS_AS bf16x8*)(lds + fa[pass & 1][q >> 3] + (pass >> 1) * XIMG + (q & 7) * 2176);
  };
  unsigned U[60][4][2], V[60][4][2];
  u32x4 B[60][4];
  auto mkU = [&](int s, int r, int j) { U[s][r][j] = xsel<MODD>(raw[s][r][j], raw[s][r][2 + j]); };
  auto mkV = [&](int s, int r, int j) { V[s][r][j] = xsel<~MODD>(raw[s][r][2 + j], raw[s][r][j]); };
  auto mkB = [&](int s, int e, int d) {
    const unsigned sel = (e & 1) ? 0x07060302u : 0x05040100u;
    B[s][e][d] = d < 2 ? __builtin_amdgcn_perm(V[s][2 * d + 1][e >> 1], V[s][2 * d][e >> 1], sel)
                       : __builtin_amdgcn_perm(U[s][2 * d - 3][e >> 1], U[s][2 * d - 4][e >> 1], sel);
  };

  f32x4 acc[8][4];
  bf16x8 F[2][15];
#pragma unroll
  for (int i = 0; i < 8; ++i) mkU(0, i >> 1, i & 1);
#pragma unroll
  for (int i = 0; i < 8; ++i) mkV(0, i >> 1, i & 1);
#pragma unroll
  for (int i = 0; i < 16; ++i) mkB(0, i >> 2, i & 3);
  SCHED();
#pragma unroll
  for (int q = 0; q <= 8; ++q) {
    if (q < 8) { stX(0, 2 * q, xs[2 * q]); stX(0, 2 * q + 1, xs[2 * q + 1]); F[0][q] = ldF(0, q); }
#pragma unroll
    for (int e = 0; e < 4; ++e) {
      SCHED();
      if (q > 0) mfma0(acc[q - 1][e], B[0][e], F[0][q - 1]);
      SCHED();
      if (e < NQ) ldS(NPRE + NQ * q + e);
    }
  }
  static_assert(NPRE + NQ * 9 >= 21 + 4 * (RING - 1), "");
  xo0 += 1024; xo1 += 1024;
  SCHED();
#pragma unroll
  for (int i = 0; i < 8; ++i) mkU(1, i >> 1, i & 1);
#pragma unroll
  for (int i = 0; i < 8; ++i) mkV(1, i >> 1, i & 1);
  stX(0, 16, xs[16]);
  F[0][8] = ldF(0, 8);
#pragma unroll
  for (int i = 0; i < 16; ++i) mkB(1, i >> 2, i & 3);
  SCHED();
#pragma unroll
  for (int i = 0; i < 4; ++i) ldW(wcnt(0) + i);
  xs[0] = ldX(0);
  if (xq(XD, 1) >= 0) xs[1] = ldX(1);
  pf = __builtin_amdgcn_raw_buffer_load_b32(rw, pfw, 0, 0);
  unsigned cpf;
  {
    unsigned long long pc;
    unsigned len;
    asm volatile("s_getpc_b64 %0\n\ts_mov_b32 %1, .Lconv_code_end-." : "=s"(pc), "=s"(len));
    const __amdgpu_buffer_rsrc_t rc = __builtin_amdgcn_make_buffer_rsrc((void*)pc, 0, len, 0x00020000);
    cpf = __builtin_amdgcn_raw_buffer_load_b32(rc, lane < 3 ? (unsigned)((lane * 128 + idx * 4 + wv) * 128) : 0x80000000u, 0, 0);
  }
  SCHED();
  STAMP(1);
  unsigned hch, sent;

  const int cb = n0 + 32 * (wv & 1) + 4 * (wv >> 1);
  __bf16 bi1, bi2, ga1, be1;
  unsigned ln;
  auto step = [&](auto GC) {
    constexpr int G = decltype(GC)::value, S = G % 30;
    constexpr int PS = S < 8 ? 0 : S < 16 ? 1 : S < 23 ? 2 : 3;
    constexpr int J = S - (PS == 0 ? 0 : PS == 1 ? 8 : PS == 2 ? 16 : 23), NT = PS < 2 ? 8 : 7;
    constexpr int W0 = xq(G, 0), W1 = xq(G, 1), L0 = xq(G + XD, 0), L1 = xq(G + XD, 1);
    constexpr int WA = wcnt(G), WN = wcnt(G + 1) - WA;
    constexpr unsigned HM = hmask(G), WM = wmask(G);
    constexpr int G0 = G & ~1, N0 = (G + 1) & ~1, NJ = (G + 1) & 1, JN = J - NT + 4 + (PS == 2);
    static_assert(xq(G, 2) < 0, "");
    static_assert(W0 < 0 || G < ((W0 >> 5) == 0 ? 12 : (W0 >> 5) == 1 ? 26 : 42) + ((W0 & 31) >> 2), "");
    static_assert(W1 < 0 || G < ((W1 >> 5) == 0 ? 12 : (W1 >> 5) == 1 ? 26 : 42) + ((W1 & 31) >> 2), "");
    static_assert( WN >= 0 && WN <= 6, "");
#pragma unroll
    for (int k = 0; k < 32; ++k) {
      const int e = G == 1 ? k >> 3 : 2 * (G & 1) + (k >> 4), kk = (k & 16) ? 31 - k : k;
      const int m = G == 1 ? k & 7 : kk >> 1, sj = G == 1 ? 1 : G0 + (kk & 1);
      SCHED();
      mfma(acc[m][e], B[sj][e], F[pasof(sj) & 1][m + posof(sj)]);
      SCHED();
#pragma unroll
      for (int u = vcum(HM, k); u < vcum(HM, k + 1); ++u) {
        const int sn = N0 + (u >> 4), t = u & 15;
        if (t >= 8) mkB(sn, 2 * NJ + ((t >> 2) & 1), t & 3);
        else if ((t < 4) == (u < 16)) mkU(sn, UREV ^ (t & 3), NJ);
        else mkV(sn, UREV ^ (t & 3), NJ);
      }
      if (G >= 53 && ((k & 1) || k >= 24)) {
        const int sn = G == 53 ? 56 : G + 4, j = G == 53;
        if (k < 4 || (k >= 20 && k < 24)) mkU(sn, (k >> 1) & 3, j);
        else if (k < 8 || (k >= 16 && k < 20)) mkV(sn, (k >> 1) & 3, j);
        else if (k >= 24) mkB(sn, 2 * j + (k & 1), (k >> 1) & 3);
      }
      if (k == KF && J == 0) F[PS & 1][8] = ldF(PS, 8);
      if (k == KF + (J == 0 ? 2 : 0) && J + 2 < NT + (G == 1)) F[PS & 1][(9 + J - (G == 1)) % 15] = ldF(PS, 9 + J - (G == 1));
      if (G == 1 && k == KF + 2) F[0][10] = ldF(0, 10);
      if (G == 55 && (k == KF + 2 || k == KF + 4)) F[1][11 + (k >> 1)] = ldF(3, 11 + (k >> 1));
      if (G < 53 && (k == KF + 2 || k == KF + 4) && JN >= 0 && JN < 4) F[(PS + 1) & 1][(2 * JN + (k == KF + 4)) & 7] = ldF((PS + 1) & 3, 2 * JN + (k == KF + 4));
      if (k == KS0 && W0 >= 0) stX((W0 >> 5) != 1, W0 & 31, xs[W0 & 31]);
      if (k == KS1 && W1 >= 0) stX((W1 >> 5) != 1, W1 & 31, xs[W1 & 31]);
      if (k == KL0 - 1 && L0 == 32) { xo0 ^= 1536; xo1 ^= 1536; if (PFM == 0) pfw ^= 262144; }
      if (k == KL0 - 1 && L0 == 64) { xo0 += 1024; xo1 += 1024; }
      if (k == KL0 && L0 >= 0) xs[L0 & 31] = ldX(L0 & 31);
      if (k == KL1 && L1 >= 0) xs[L1 & 31] = ldX(L1 & 31);
      if ((WM >> k) & 1) ldW(WA + __builtin_popcount(WM & ((1u << k) - 1)));
      if (k == 31 && (S == PF1 || G == PF2)) {
        asm volatile("" :: "v"(pf));
        pf = __builtin_amdgcn_raw_buffer_load_b32(rw, pfw, S == PF1 ? D * D * 2 : 0, 0);
      }
      if (G == HS && k == 29) {
        hch = (unsigned)(hr >> 32) + 1u;
        __hip_atomic_store(mout, (hr << 32) | hch, RLX_AGENT);
      }
      if (G == 5 && k == 30) asm volatile("" :: "v"(cpf));
      if (G == 51 && k == 30) hr = __hip_atomic_load(min_, RLX_AGENT);
      if (G == 54 && k == 30) {
        asm volatile("" :: "v"(pf));
        __hip_atomic_store(mout, (hr << 32) | hch, RLX_AGENT);
        sent = (unsigned)hr;
      }
      if (G == 52 && k == 16) {
        static_assert(wcnt(52) == 240, "");
        asm volatile("v_mbcnt_lo_u32_b32 %0, -1, 0\n\tv_mbcnt_hi_u32_b32 %0, -1, %0" : "=v"(ln));
        bi1 = bias[cb + 8 * (ln >> 4) + (ln & 3)]; bi2 = bias[cb + 8 * (ln & 3) + (ln >> 4)]; ga1 = gamma[cb + 8 * (ln & 3) + (ln >> 4)]; be1 = beta[cb + 8 * (ln & 3) + (ln >> 4)];
      }
    }
  };
#define ST(n) step(std::integral_constant<int, n>{})
#define ST10(n) ST(n); ST(n + 1); ST(n + 2); ST(n + 3); ST(n + 4); ST(n + 5); ST(n + 6); ST(n + 7); ST(n + 8); ST(n + 9)
  ST(1); ST(2); ST(3); ST(4); ST(5); ST(6); ST(7); ST(8); ST(9);
  ST10(10); ST10(20); ST10(30); ST10(40); ST(50); ST(51); ST(52); ST(53); ST(54); ST(55);
  unsigned la[4], gpub, go;
  const __amdgpu_buffer_rsrc_t rg = __builtin_amdgcn_make_buffer_rsrc((void*)gran, 0, GRAN_BYTES, 0x00020000);
  auto stA = [&](int a) { *(LDS_AS f32x4*)(xa0 + ((a & 7) * 4 + (a >> 3)) * 1088) = acc[a & 7][a >> 3]; };
#define TI 1
#pragma unroll
  for (int n4 = 0; n4 < 32; ++n4)
#pragma unroll
  for (int n = 4 * n4; n < 4 * n4 + 4; ++n) {
    const int a = (n / (4 * TI)) * TI + n % TI, j = ((n / TI) & 3) ^ ((n & 4) ? 3 : 0);
    const int e = a >> 3, m = a & 7, k = n & 31;
    SCHED();
    mfma(acc[m][e], B[56 + j][e], F[1][m + 3 + j]);
    SCHED();
    if (e == 0 && k < 12) mkU(57 + (k >> 2), k & 3, 1);
    if (e == 0 && k >= 12 && k < 24) mkV(57 + ((k - 12) >> 2), k & 3, 1);
    if (e == 1 && k < 24) mkB(57 + (k % 12 >> 2), 2 + k / 12, k & 3);
    if ((n & 3) == 3 && (n >> 2) >= TI + 1) stA((n >> 2) - TI - 1);
    if (n == 88) hr = __hip_atomic_load(min_, RLX_AGENT);
    if (n == 72) {
      go = (unsigned)(((grp * 16 + (ln & 12)) * 64 + wv * 16 + 4 * (ln & 3) + (ln >> 4)) * 8);
      la[0] = (ln & 3) * 1088 + wv * 256; la[1] = la[0] + 2 * WREG; la[2] = la[0] + (ln >> 2) * 16; la[3] = la[2] + 2 * WREG;
      gpub = (ln & 12) ? 0x80000000u : (unsigned)(((grp * 16 + (mt & 15)) * 64 + wv * 16) * 8) + ((ln >> 2) + (ln & 3)) * 8;
    }
  }
  STAMP(2);
  asm volatile("s_nop 7" ::: "memory");
  SCHED();
#pragma unroll
  for (int a = 31 - TI; a < 32; ++a) stA(a);
  asm volatile("s_waitcnt lgkmcnt(0)\n\ts_barrier" : "+v"(la[0]), "+v"(la[1]), "+v"(la[2]), "+v"(la[3]), "+v"(gpub), "+v"(go) :: "memory");

  f32x4 v[8], piv, d0, s1, s2;
  float pv;
  bool hok;
  {
    f32x4 p[8][4], pz[4];
#pragma unroll
    for (int ww = 0; ww < 4; ++ww) pz[ww] = *(const LDS_AS f32x4*)(lds + la[ww >> 1] + (ww & 1) * WREG);
#pragma unroll
    for (int m = 0; m < 3; ++m)
#pragma unroll
      for (int ww = 0; ww < 4; ++ww) p[m][ww] = *(const LDS_AS f32x4*)(lds + la[2 + (ww >> 1)] + (ww & 1) * WREG + m * 4352);
    SCHED();
    piv = (pz[0] + pz[1]) + (pz[2] + pz[3]);
#pragma unroll
    for (int m = 0; m < 8; ++m) {
      if (m + 3 < 8) {
#pragma unroll
        for (int ww = 0; ww < 4; ++ww) p[(m + 3) & 7][ww] = *(const LDS_AS f32x4*)(lds + la[2 + (ww >> 1)] + (ww & 1) * WREG + (m + 3) * 4352);
      }
      SCHED();
      v[m] = (p[m][0] + p[m][1]) + (p[m][2] + p[m][3]);
      const f32x4 d = v[m] - piv;
      if (m == 0) {
        d0 = d;
        const float p01 = (ln & 16) ? piv[1] : piv[0], p23 = (ln & 16) ? piv[3] : piv[2];
        pv = ((ln & 32) ? p23 : p01) + (float)bi1;
        asm volatile("" : "+v"(pv));
        hok = __all((unsigned)(hr >> 32) == hch && (unsigned)hr == sent);
      }
      else { s1 = m == 1 ? d : s1 + d; s2 = m == 1 ? d * d : s2 + d * d; }
      SCHED();
    }
  }
  STAMP(3);
  const bool last = (mt & 15) == 15;
  float mown;
  {
    if (last) {
      const f32x4 z = {0.f, 0.f, 0.f, 0.f};
      asm volatile("");
      if (ln >= 40) d0 = z;
      if (ln >= 36) { s1 = z; s2 = z; }
    }
    s1 += d0; s2 += d0 * d0;
    float a1 = swadd16(swadd32(s1[0], s1[2]), swadd32(s1[1], s1[3]));
    float a2 = swadd16(swadd32(s2[0], s2[2]), swadd32(s2[1], s2[3]));
    a1 += dpp<0x124>(a1); a2 += dpp<0x124>(a2);
    a1 += dpp<0x128>(a1); a2 += dpp<0x128>(a2);
    const float dm = a1 * (last ? 1.0f / (float)(L - 15 * 128) : 1.0f / 128.0f);
    mown = pv + dm;
    unsigned us = __builtin_bit_cast(unsigned, a2 - a1 * dm);
    if (us == INVALID) us = 0x7FC00000u;
    __builtin_amdgcn_raw_buffer_store_b64(u32x2{__builtin_bit_cast(unsigned, mown), us}, rg, gpub, 0, 16);
  }
  STAMP(7);
  if (!hok) {
#pragma nounroll
    for (int sp = 0; sp < SPIN_CAP; ++sp) {
      if (__any((unsigned)hr != sent)) { __hip_atomic_store(mout, (hr << 32) | hch, RLX_AGENT); sent = (unsigned)hr; }
      if (__all((unsigned)(hr >> 32) == hch)) break;
      __builtin_amdgcn_s_sleep(1);
      hr = __hip_atomic_load(min_, RLX_AGENT);
    }
    asm volatile("" :: "v"(hr));
  }
    asm volatile("" ::: "memory");
    u32x2 q[4];
    auto poll = [&](u32x2* r) {
#pragma unroll
      for (int j = 0; j < 4; ++j) r[j] = __builtin_bit_cast(u32x2, __builtin_amdgcn_raw_buffer_load_b64(rg, go + j * 512, 0, 16));
    };
    auto bad = [&](const u32x2* r) { return __any(max(max(r[0][1], r[1][1]), max(r[2][1], r[3][1])) == INVALID); };
  poll(q);
  {
    u32x4 a[8], t[8];
#pragma unroll
    for (int m = 0; m < 8; ++m) { a[m] = __builtin_bit_cast(u32x4, v[m]); t[m][0] = xsel<0x5555555555555555ull, 1>(a[m][1], a[m][0]); t[m][2] = xsel<0x5555555555555555ull, 1>(a[m][3], a[m][2]); }
#pragma unroll
    for (int m = 0; m < 8; ++m) { t[m][1] = xsel<0xAAAAAAAAAAAAAAAAull, 1>(a[m][0], a[m][1]); t[m][3] = xsel<0xAAAAAAAAAAAAAAAAull, 1>(a[m][2], a[m][3]); }
#pragma unroll
    for (int m = 0; m < 8; ++m) { a[m][0] = xsel<0x3333333333333333ull, 2>(t[m][2], t[m][0]); a[m][1] = xsel<0x3333333333333333ull, 2>(t[m][3], t[m][1]); }
#pragma unroll
    for (int m = 0; m < 8; ++m) { a[m][2] = xsel<0xCCCCCCCCCCCCCCCCull, 2>(t[m][0], t[m][2]); a[m][3] = xsel<0xCCCCCCCCCCCCCCCCull, 2>(t[m][1], t[m][3]); }
#pragma unroll
    for (int m = 0; m < 8; ++m) v[m] = __builtin_bit_cast(f32x4, a[m]);
  }
  const float pm = __builtin_bit_cast(float, __builtin_amdgcn_ds_bpermute(((ln & 3) * 16 + (ln >> 4)) * 4, __builtin_bit_cast(int, mown)));
  f32x4 sc, sh;
  {
    float gaf = (float)ga1, bef = (float)be1, pmb = pm - (float)bi2, n3 = (ln & 12) != 12 ? 128.f : (float)(L - 15 * 128);
    asm volatile("" : "+v"(gaf), "+v"(bef), "+v"(pmb), "+v"(n3));
#pragma nounroll
    for (int sp = 0; sp < SPIN_CAP; ++sp) {
      if (!bad(q)) break;
      __builtin_amdgcn_s_sleep(1);
      asm volatile("" ::: "memory");
      poll(q);
    }
    float d[4], m2[4];
#pragma unroll
    for (int j = 0; j < 4; ++j) { const f32x2 f = __builtin_bit_cast(f32x2, q[j]); d[j] = f[0] - pm; m2[j] = f[1]; }
    const float e3 = n3 * d[3];
    float a1 = 128.f * ((d[0] + d[1]) + d[2]) + e3;
    float a2 = 128.f * (d[0] * d[0] + d[1] * d[1] + d[2] * d[2]) + (e3 * d[3] + ((m2[0] + m2[1]) + (m2[2] + m2[3])));
    a1 += dpp<0x124>(a1); a2 += dpp<0x124>(a2);
    a1 += dpp<0x128>(a1); a2 += dpp<0x128>(a2);
    const float dm = a1 * (1.0f / (float)L);
    const float var = a2 * (1.0f / (float)L) - dm * dm;
    const float scl = __builtin_amdgcn_rsqf(var + 1e-5f) * gaf;
    const float shf = bef - (pmb + dm) * scl;
    sc = rows4(scl); sh = rows4(shf);
  }
  STAMP(4);
  const __amdgpu_buffer_rsrc_t ro = __builtin_amdgcn_make_buffer_rsrc((void*)(out + (size_t)b * L * D), 0, L * D * 2, 0x00020000);
  const unsigned oo = (unsigned)((l0 + 8 * (ln >> 2)) * (D * 2) + cb * 2 + (ln & 3) * 16);
#pragma unroll
  for (int m = 0; m < 8; ++m) {
    const f32x4 u = v[m] * sc + sh;
    const f32x4 t = u * (u * u * -0.1029432397f - 2.3022081985f);
    f32x4 r;
#pragma unroll
    for (int k = 0; k < 4; ++k) r[k] = __builtin_amdgcn_exp2f(t[k]);
    r += 1.0f;
#pragma unroll
    for (int k = 0; k < 4; ++k) r[k] = __builtin_amdgcn_rcpf(r[k]);
    r *= u;
    __builtin_amdgcn_raw_buffer_store_b64(__builtin_bit_cast(u32x2, bf16x4{(__bf16)r[0], (__bf16)r[1], (__bf16)r[2], (__bf16)r[3]}), ro, oo + m * 1024, 0, 0);
  }
  STAMP(5);
  asm volatile(".Lconv_code_end:");
}

extern "C" void kernel_launch(void* const* d_in, const int* in_sizes, int n_in,
                              void* d_out, int out_size, void* d_ws, size_t ws_size,
                              hipStream_t stream) {
  const __bf16* x = (const __bf16*)d_in[0];
  const __bf16* w = (const __bf16*)d_in[1];
  const __bf16* bias = (const __bf16*)d_in[2];
  const __bf16* gamma = (const __bf16*)d_in[3];
  const __bf16* beta = (const __bf16*)d_in[4];
  unsigned long long* gran = (unsigned long long*)d_ws;
  unsigned long long* stamps = (unsigned long long*)((char*)d_ws + (size_t)32 * 1024 * 1024);
  conv_gn_gelu_kernel<<<256, 256, 0, stream>>>(x, w, bias, gamma, beta, (__bf16*)d_out, gran, stamps);
}
```

```cpp
#include <hip/hip_runtime.h>
#include <type_traits>
#include <utility>

typedef __bf16 bf16x2 __attribute__((ext_vector_type(2)));
typedef __bf16 bf16x4 __attribute__((ext_vector_type(4)));
typedef __bf16 bf16x8 __attribute__((ext_vector_type(8)));
typedef float f32x4 __attribute__((ext_vector_type(4)));
typedef float f32x2 __attribute__((ext_vector_type(2)));
typedef unsigned u32x2 __attribute__((ext_vector_type(2)));
typedef unsigned u32x3 __attribute__((ext_vector_type(3)));
typedef unsigned u32x4 __attribute__((ext_vector_type(4)));
#define LDS_AS __attribute__((address_space(3)))
#define GAS __attribute__((address_space(1)))
#define DI __device__ __forceinline__

constexpr int T = 4000, D = 512, KT = 15, L = 1993;
constexpr int XPL = 544, XRES = 4 * XPL, XIMG = 8 * XRES, WREG = 2 * XIMG;
constexpr int LDS_BYTES = 4 * WREG;
constexpr int API = 8256, PREG = WREG + 16;
constexpr int SPIN_CAP = 1 << 20;
constexpr unsigned INVALID = 0xFFFFFFFFu;
constexpr int GST = 128;
constexpr int GRAN_BYTES = 4096 * GST;

#define RLX_AGENT __ATOMIC_RELAXED, __HIP_MEMORY_SCOPE_AGENT
#define SCHED() __builtin_amdgcn_sched_barrier(0)
#define FAS 46
#define PF1 6
#define PF2 20
#define PF3 36
#define GB 43
#define RB 4
#define SLP 1
#define HG 46
#define HA 12
#define OAUX 0
#define XPF 0
#define NX 0
constexpr int TS = 52;
constexpr int SU = 42;
#define RING 3
#define NPRE 8
#define NQ 3
#define HS 3
#define XD 3
#define CO 4
#define QW 1
constexpr unsigned woff(int S) {
  return S < 16 ? (unsigned)((2 * (S & 7)) * (D * D * 2) + (S >> 3) * 32768)
                : (unsigned)((2 * ((S - 16) % 7) + 1) * (D * D * 2) + ((S - 16) / 7) * 32768);
}
constexpr int pasof(int G) { return G % 30 < 8 ? 0 : G % 30 < 16 ? 1 : G % 30 < 23 ? 2 : 3; }
constexpr int posof(int G) { return G % 30 - (pasof(G) == 0 ? 0 : pasof(G) == 1 ? 8 : pasof(G) == 2 ? 16 : 23); }
constexpr bool quiet(int G) { return (G > PF1 && G <= PF1 + QW) || (G > PF2 && G <= PF2 + QW) || (G > PF3 && G <= PF3 + QW); }
constexpr int xst(int im, int p) {
  const int a = im == 0 ? XD : im == 1 ? 16 : 30, z = im == 0 ? 15 : im == 1 ? 29 : 45;
  int n = 0;
  for (int g = a; g <= z; ++g) n += !quiet(g - XD);
  int i = (p * (n - 1) + 8) / 16;
  for (int g = a; g <= z; ++g)
    if (!quiet(g - XD) && i-- == 0) return g;
  return -1;
}
constexpr int xq(int G, int n) {
  for (int im = 0; im < 3; ++im)
    for (int p = 0; p < 17; ++p)
      if (xst(im, p) == G && n-- == 0) return im * 32 + p;
  return -1;
}
constexpr int wcnt(int G) {
  int n = 4 * (G + RING + 1) + (G > GB ? RB * (G - GB) : 0);
  for (int i = 0; i < 3; ++i) {
    const int d = G - 1 - (i == 0 ? PF1 : i == 1 ? PF2 : PF3);
    n += d <= 0 ? (4 * QW + d > 0 ? 4 * QW + d : 0) : (4 * QW - 4 * d > 0 ? 4 * QW - 4 * d : 0);
  }
  return n < 240 ? n : 240;
}
constexpr unsigned wmask(int G) {
  int wn = wcnt(G + 1) - wcnt(G);
  const int pos[8] = {14, 18, 22, 26, xq(G + XD, 0) < 0 ? 12 : -1, xq(G + XD, 1) < 0 ? 28 : -1, 30, 24};
  unsigned m = 0;
  for (int i = 0; i < 8; ++i)
    if (pos[i] >= 0 && wn > 0) { m |= 1u << pos[i]; --wn; }
  return m;
}
constexpr int fq(int G, int k) {
  const int PS = pasof(G), J = posof(G), NT = PS < 2 ? 8 : 7, JN = J - NT + 4 + (PS == 2);
  if (G == 1) return k == 0 ? 9 : k == 2 ? 10 : -1;
  if (J == 0 && k == 0) return 8;
  if (k == (J == 0 ? 2 : 0) && J + 2 < NT) return 9 + J;
  if ((k == 2 || k == 4) && JN >= 0 && JN < 4) return 16 + 2 * JN + (k == 4);
  return -1;
}
struct Tab { int v[512]; };
constexpr Tab slist() {
  Tab t{};
  int n = 0;
  for (int h = 0; h < 2 * (TS - SU) + NX + 58 - TS; ++h) {
    const int x = h - 2 * (TS - SU), G = SU + (h >> 1);
    const int s = x < 0 ? ((G + 1) & ~1) + (h & 1) : x < NX ? TS + x : TS + 2 + x - NX, j = x < 0 ? (G + 1) & 1 : x < NX;
    if (!j) t.v[++n] = s * 64 + 16;
    for (int i = 0; i < 16; ++i) t.v[++n] = s * 64 + j * 32 + i;
  }
  t.v[0] = n;
  return t;
}
constexpr Tab SL = slist();
constexpr Tab scum() {
  Tab t{};
  int n = SL.v[0];
  t.v[(TS - SU) * 32] = n;
  for (int G = TS - 1; G >= SU; --G) {
    const unsigned wm = wmask(G) | (xq(G, 0) >= 0 ? 0x154u : 0u) | (xq(G, 1) >= 0 ? 0x1110000u : 0u) | (wmask(G) ? 0x400u : 0u) |
                        (G == TS - 2 ? 0x14111000u : 0u) | (G == HG ? 1u << 20 : 0u) | (xq(G + XD, 0) >= 0 ? 1u << 12 : 0u) | (xq(G + XD, 1) >= 0 ? 1u << 28 : 0u);
    for (int k = 31; k >= 0; --k) {
      n -= (k & 1) ? 2 : ((wm >> k) & 1) ? 0 : 2 - (fq(G, k) >= 0) - (k == (posof(G) == 0 ? 8 : 30));
      const int o = (G - SU) * 33 - (G & 1) + (k & ~1) + ((G & 1) ? (k > 0) + (k > 16) : 0);
      if (n < o) n = o;
      t.v[(G - SU) * 32 + k] = n;
    }
  }
  return t;
}
constexpr int flast(int G) { return fq(G, 4) >= 0 ? fq(G, 4) : fq(G, 2) >= 0 ? fq(G, 2) : fq(G, 0); }
constexpr int tocc(int a, int j) {
  if (a == 0) return j < 6;
  if (j >= 2 && j <= 5) return 2;
  if (a < 9) return a == 1 && j == 7;
  if (a == HA && j == 1) return 2;
  const int occ[32] = {0, 2, 1, 1, 1, 1, 1, 2, 2, 2, 2, 2, 0, 0, 1, 2, 0, 2, 1, 1, 0, 1, 2, 2, 0, 2, 1};
  return occ[((a - 9) & 7) * 4 + (j & 3)];
}
constexpr Tab tcum() {
  Tab t{};
  for (int u = 0; u < 256; ++u) t.v[u + 1] = t.v[u] + 2 - tocc(u >> 3, u & 7);
  return t;
}
constexpr Tab SC = scum(), TC = tcum();
static_assert(SC.v[0] == 0 && !(SU & 1), "");
template <int A, int... I, class FT> DI void sfor_(std::integer_sequence<int, I...>, FT&& f) { (f(std::integral_constant<int, A + I>{}), ...); }
template <int A, int Z, class FT> DI void sfor(FT&& f) { sfor_<A>(std::make_integer_sequence<int, Z - A>{}, f); }

DI unsigned xsel(unsigned oth, unsigned own) {
  unsigned r;
  register unsigned long long m asm("vcc") = 0xFF00FF00FF00FF00ull;
  asm volatile("v_cndmask_b32_dpp %0, %1, %2, vcc row_ror:8 row_mask:0xf bank_mask:0xf" : "=v"(r) : "v"(oth), "v"(own), "s"(m));
  return r;
}
template <bool FA>
DI void mfma(f32x4& acc, const u32x4& a, const bf16x8& b) {
  if (FA) asm volatile("v_mfma_f32_16x16x32_bf16 %0, %1, %2, %0" : "+a"(acc) : "v"(a), "a"(b));
  else asm volatile("v_mfma_f32_16x16x32_bf16 %0, %1, %2, %0" : "+a"(acc) : "v"(a), "v"(b));
}
DI void mfma0(f32x4& acc, const u32x4& a, const bf16x8& b) {
  asm volatile("v_mfma_f32_16x16x32_bf16 %0, %1, %2, 0" : "=a"(acc) : "v"(a), "v"(b));
}
DI void setm0(unsigned v) { asm volatile("s_mov_b32 m0, %0" :: "s"(v) : "memory"); }
DI void st_tid(unsigned v, int off) { asm volatile("ds_write_addtid_b32 %0 offset:%1" :: "v"(v), "n"(off) : "memory"); }
DI void lds_barrier() {
  asm volatile("" ::: "memory");
  __builtin_amdgcn_s_waitcnt(0xC07F);
  __builtin_amdgcn_s_barrier();
  asm volatile("" ::: "memory");
}
DI unsigned fu(float v) { return __builtin_bit_cast(unsigned, v); }
DI float uf(unsigned v) { return __builtin_bit_cast(float, v); }
DI f32x2 lo2(const f32x4& v) { return f32x2{v[0], v[1]}; }
DI f32x2 hi2(const f32x4& v) { return f32x2{v[2], v[3]}; }
DI f32x2 fma2(f32x2 a, f32x2 b, f32x2 c) { return __builtin_elementwise_fma(a, b, c); }
DI float bfel(const u32x2& v, int e) { return uf((e & 1) ? v[e >> 1] & 0xFFFF0000u : v[e >> 1] << 16); }
#define PIN(x) asm volatile("" :: "v"(x))
template <int CTRL>
DI float dpp(float v) { return __builtin_bit_cast(float, __builtin_amdgcn_update_dpp(0, __builtin_bit_cast(int, v), CTRL, 0xF, 0xF, true)); }

DI void sum16(float& a, float& b) {
  auto p = __builtin_amdgcn_permlane32_swap(fu(a), fu(b), false, false);
  float v = uf(p[0]) + uf(p[1]);
  v += dpp<0x128>(v);
  v += dpp<0x124>(v);
  auto r = __builtin_amdgcn_permlane16_swap(fu(v), fu(v), false, false);
  v = uf(r[0]) + uf(r[1]);
  auto q = __builtin_amdgcn_permlane32_swap(fu(v), fu(v), false, false);
  a = uf(q[0]); b = uf(q[1]);
}

#define XST(k) do {} while (0)
#define STAMP(k) do {} while (0)

__global__ __launch_bounds__(256, 1)
void conv_gn_gelu_kernel(const __bf16* __restrict__ x, const __bf16* __restrict__ w, const __bf16* __restrict__ bias,
                         const __bf16* __restrict__ gamma, const __bf16* __restrict__ beta, __bf16* __restrict__ out,
                         unsigned long long* gran, unsigned long long* __restrict__ stamps) {
  __shared__ __attribute__((aligned(1024))) char smem[LDS_BYTES];
  LDS_AS char* const lds = (LDS_AS char*)smem;
  const int tid = threadIdx.x, lane = tid & 63;
  const int wv = __builtin_amdgcn_readfirstlane(tid >> 6);
  const int bid = blockIdx.x;
  const int xcd = bid & 7, idx = bid >> 3;
  const int mt = (xcd & 1) * 16 + (idx & 15), nt = (xcd >> 1) * 2 + (idx >> 4);
  const int b = mt >> 4, l0 = (mt & 15) * 128, n0 = nt * 64;
  const int c = lane & 15, g = lane >> 4;
  STAMP(0);

  const __amdgpu_buffer_rsrc_t rx = __builtin_amdgcn_make_buffer_rsrc((void*)(x + (size_t)b * T * D), 0, (2 * L + KT - 2) * D * 2, 0x00020000);
  const __amdgpu_buffer_rsrc_t rw = __builtin_amdgcn_make_buffer_rsrc((void*)w, 0, KT * D * D * 2, 0x00020000);

  const int grp = b * 8 + nt;
  const __amdgpu_buffer_rsrc_t rg = __builtin_amdgcn_make_buffer_rsrc((void*)gran, 0, GRAN_BYTES, 0x00020000);
  const int gbase = (grp * 4 + wv) * 64 * GST;
  GAS unsigned long long* const mbase = (GAS unsigned long long*)gran + GRAN_BYTES / 8 + (grp * 4 + wv) * 256;
  GAS unsigned long long* const min_ = mbase + (mt & 15) * 16 + c;
  GAS unsigned long long* const mout = mbase + c * 16 + (mt & 15);

  const unsigned wo = (unsigned)((2 * g + 16 * (c >> 3)) * 1024 + n0 * 2 + (c & 7) * 16 + wv * 65536);
  u32x4 raw[60][4];
  unsigned so[60];
  auto ldW = [&](int n) {
    raw[n >> 2][n & 3] = __builtin_bit_cast(u32x4, __builtin_amdgcn_raw_buffer_load_b128(rw, wo + (n & 1) * 1024 + (n & 2) * 4096, so[n >> 2], 0));
  };
#pragma unroll
  for (int i = 0; i < RING + 2; ++i) so[i] = woff(i) + CO * 65536;
#pragma unroll
  for (int i = 0; i < 4; ++i) ldW(i);
  SCHED();

  unsigned xo0 = (unsigned)((2 * l0 + 16 * (lane >> 3)) * (D * 2) + ((lane ^ (lane >> 1)) & 4) * 16 + (lane & 3) * 16 + (wv + CO) * 128);
  unsigned xo1 = (unsigned)((2 * l0 + 256 + 2 * (lane >> 3)) * (D * 2) + (lane & 7) * 16 + (wv + CO) * 128);
  const unsigned xa1 = (unsigned)(wv * WREG + (lane >> 3) * XRES + 512 + (lane & 7) * 4);
  auto ldX = [&](int pc) -> u32x4 {
    return __builtin_bit_cast(u32x4, __builtin_amdgcn_raw_buffer_load_b128(rx, pc < 16 ? xo0 : xo1,
                                                                           pc < 16 ? (pc >> 1) * 2048 + (pc & 1) * 131072 : 0, 0));
  };
  auto stX = [&](int p, int pc, const u32x4& v, int d) {
    if (pc == 16) *(LDS_AS unsigned*)(lds + xa1 + p * XIMG + d * XPL) = v[d];
    else st_tid(v[d], p * XIMG + (pc >> 1) * XRES + d * XPL + (pc & 1) * 256);
  };
  u32x4 xs[32];
  unsigned long long hr;
  auto ldS = [&](int n) {
    if (n < 16) xs[n & 15] = ldX(n);
    else if (n < 20) ldW(n - 12);
    else if (n == 20) xs[16] = ldX(16);
    else if (n < 21 + 4 * (RING - 1)) {
      ldW(n - 13);
      if (n == 20 + 4 * (HS - 1)) {
        __builtin_amdgcn_raw_buffer_store_b64(u32x2{INVALID, INVALID}, rg, lane * 8, gbase + (mt & 15) * 4 * GST, 16);
        hr = __hip_atomic_load(min_, RLX_AGENT);
      }
    }
  };
#pragma unroll
  for (int i = 0; i < NPRE; ++i) { ldS(i); SCHED(); }
  setm0(wv * WREG);

  unsigned pfw = lane < 32 ? (unsigned)(((lane >> 2) * 2 * D + (mt & 15) * 4 + (lane & 3)) * (D * 2) + n0 * 2 + (wv + CO) * 65536) : 0x80000000u;

  const unsigned pfx = lane < 16 ? (unsigned)((2 * l0 + 32 * nt + 2 * lane) * (D * 2) + wv * 128) : 0x80000000u;
  unsigned px = 0;
  const int cb = n0 + 32 * (wv & 1) + 4 * (wv >> 1), ei = lane & 3, cq = c >> 2;
  const bool last = (mt & 15) == 15;
  unsigned aa = (unsigned)(wv * PREG + lane * 4);
  unsigned la0 = (unsigned)(ei * API + wv * 64 + g * 1024 + cq * 16), la1 = la0 + 2 * PREG;
  unsigned lq = (unsigned)(ei * API + wv * 64 + cq * PREG + (ei >> 1) * 4);
  unsigned gs = lane >= 4 ? 0x80000000u : (unsigned)(gbase + (mt & 15) * 4 * GST + ei * 16);
  unsigned go = (unsigned)(gbase + (lane >> 2) * 4 * GST + ei * 16);
  unsigned oo = (unsigned)((l0 + 32 * cq + g) * (D * 2) + (cb + 8 * ei) * 2);
  f32x2 mA = {last && cq == 3 ? 0.f : 1.f, last && cq >= 2 && (lane & 52) ? 0.f : 1.f}, mB = {mA[0], last && cq >= 2 ? 0.f : 1.f};
  unsigned po = (unsigned)((cb + 8 * ei) * 2);
  float nrow = lane >= 60 ? (float)(L - 15 * 128) : 128.f;
  asm volatile("" : "+v"(po), "+v"(nrow), "+v"(aa), "+v"(la0), "+v"(la1), "+v"(lq), "+v"(gs), "+v"(go), "+v"(oo), "+v"(mA), "+v"(mB));

  unsigned fa[2][2];
#pragma unroll
  for (int hi = 0; hi < 2; ++hi) {
    const int v = c + hi;
    fa[0][hi] = (unsigned)(wv * WREG + g * XPL + v * 32 + (v & 1) * 16);
    fa[1][hi] = fa[0][hi] ^ 16u;
  }
  auto ldF = [&](int pass, int q) -> bf16x8 {
    return *(LDS_AS bf16x8*)(lds + fa[pass & 1][q >> 3] + (pass >> 1) * XIMG + (q & 7) * XRES);
  };
  unsigned U[60][4][2], V[60][4][2];
  u32x4 B[60][4];
  auto mkU = [&](int s, int r, int j) { U[s][r][j] = xsel(raw[s][r][j], raw[s][r][2 + j]); };
  auto mkV = [&](int s, int r, int j) { V[s][r][j] = __builtin_amdgcn_update_dpp(raw[s][r][j], raw[s][r][2 + j], 0x128, 0xF, 0xC, false); };
  auto mkB = [&](int s, int e, int d) {
    const unsigned sel = (e & 1) ? 0x07060302u : 0x05040100u;
    B[s][e][d] = d < 2 ? __builtin_amdgcn_perm(V[s][2 * d + 1][e >> 1], V[s][2 * d][e >> 1], sel)
                       : __builtin_amdgcn_perm(U[s][2 * d - 3][e >> 1], U[s][2 * d - 4][e >> 1], sel);
  };

  f32x4 acc[8][4];
  bf16x8 F[2][15];
#pragma unroll
  for (int i = 0; i < 8; ++i) mkU(0, i >> 1, i & 1);
  SCHED();
#pragma unroll
  for (int i = 0; i < 8; ++i) mkV(0, i >> 1, i & 1);
#pragma unroll
  for (int i = 0; i < 16; ++i) mkB(0, i >> 2, i & 3);
  SCHED();
#pragma unroll
  for (int q = 0; q <= 8; ++q) {
    if (q < 8) {
#pragma unroll
      for (int i = 0; i < 8; ++i) stX(0, 2 * q + (i >> 2), xs[2 * q + (i >> 2)], i & 3);
      F[0][q] = ldF(0, q);
    }
#pragma unroll
    for (int e = 0; e < 4; ++e) {
      SCHED();
      if (q > 0) mfma0(acc[q - 1][e], B[0][e], F[0][q - 1]);
      SCHED();
      if (e < NQ) ldS(NPRE + NQ * q + e);
    }
  }
  static_assert(NPRE + NQ * 9 >= 21 + 4 * (RING - 1), "");
  xo0 += 1024; xo1 += 1024;
  SCHED();
#pragma unroll
  for (int i = 0; i < 8; ++i) mkU(1, i >> 1, i & 1);
  SCHED();
#pragma unroll
  for (int i = 0; i < 8; ++i) mkV(1, i >> 1, i & 1);
#pragma unroll
  for (int i = 0; i < 4; ++i) stX(0, 16, xs[16], i);
  F[0][8] = ldF(0, 8);
#pragma unroll
  for (int i = 0; i < 16; ++i) mkB(1, i >> 2, i & 3);
  SCHED();
#pragma unroll
  for (int i = 0; i < 4; ++i) ldW(wcnt(0) + i);
  xs[0] = ldX(0);
  if (xq(XD, 1) >= 0) xs[1] = ldX(1);
  unsigned pf = __builtin_amdgcn_raw_buffer_load_b32(rw, pfw, 0, 0), cpf;
  if (XPF & 1) px = __builtin_amdgcn_raw_buffer_load_b32(rx, pfx, CO * 128 + 1024, 0);
  {
    unsigned long long pc;
    unsigned len;
    asm volatile("s_getpc_b64 %0\n\ts_mov_b32 %1, .Lconv_code_end-." : "=s"(pc), "=s"(len));
    const __amdgpu_buffer_rsrc_t rc = __builtin_amdgcn_make_buffer_rsrc((void*)pc, 0, len, 0x00020000);
    cpf = __builtin_amdgcn_raw_buffer_load_b32(rc, lane < 3 ? (unsigned)((lane * 128 + idx * 4 + wv) * 128) : 0x80000000u, 0, 0);
  }
  u32x2 bi4, ga4, be4;
  SCHED();
  STAMP(1);
  unsigned hch;
  unsigned long long ho;

  auto shuf = [&](int s, int j, int t) {
    if (t >= 8) mkB(s, 2 * j + ((t >> 2) & 1), t & 3);
    else if (t & 1) mkV(s, t >> 1, j);
    else mkU(s, t >> 1, j);
  };
  auto step = [&](auto GC) {
    constexpr int G = decltype(GC)::value;
    constexpr int PS = pasof(G), J = posof(G);
    constexpr int W0 = xq(G, 0), W1 = xq(G, 1), L0 = xq(G + XD, 0), L1 = xq(G + XD, 1);
    constexpr int WA = wcnt(G), WN = wcnt(G + 1) - WA;
    constexpr unsigned WM = wmask(G);
    constexpr int G0 = G & ~1, N0 = (G + 1) & ~1, NJ = (G + 1) & 1;
    static_assert(xq(G, 2) < 0 && WN >= 0 && WN == __builtin_popcount(WM), "");
    static_assert(W0 < 0 || G < ((W0 >> 5) == 0 ? 12 : (W0 >> 5) == 1 ? 26 : 42) + ((W0 & 31) >> 2), "");
    static_assert(W1 < 0 || G < ((W1 >> 5) == 0 ? 12 : (W1 >> 5) == 1 ? 26 : 42) + ((W1 & 31) >> 2), "");
    static_assert(wcnt(TS - 1) == 240, "");

    sfor<0, 32>([&](auto KC) {
      constexpr int k = decltype(KC)::value, FQ = fq(G, k), FL = flast(G);
      constexpr int e = G == 1 ? k >> 3 : 2 * (G & 1) + (k >> 4), kk = (k & 16) ? 31 - k : k;
      constexpr int m = G == 1 ? k & 7 : kk >> 1, sj = G == 1 ? 1 : G0 + (kk & 1), fp = pasof(sj) & 1, fi = m + posof(sj);
      SCHED();
      mfma<(sj >= FAS)>(acc[m][e], B[sj][e], F[fp][fi]);
      SCHED();
      if ((k & 1) && G < SU) { shuf(N0 + (k >> 4), NJ, (k - 1) & 15); shuf(N0 + (k >> 4), NJ, k & 15); }
      if constexpr (FQ >= 0) F[(PS + (FQ >> 4)) & 1][FQ & 15] = ldF((PS + (FQ >> 4)) & 3, FQ & 15);
      if ((k == 0 || k == 16) && NJ == 0 && G < SU) PIN(raw[N0 + (k >> 4)][3]);
      if (k >= 2 && k <= 8 && !(k & 1) && W0 >= 0) stX((W0 >> 5) != 1, W0 & 31, xs[W0 & 31], (k - 2) >> 1);
      if ((k == 10 || k == 16 || k == 20 || k == 24) && W1 >= 0) stX((W1 >> 5) != 1, W1 & 31, xs[W1 & 31], k == 10 ? 0 : (k - 12) >> 2);
      if (k == 10) {
#pragma unroll
        for (int s = (WA + 3) >> 2; 4 * s < WA + WN; ++s) asm volatile("s_mov_b32 %0, %1" : "=s"(so[s]) : "n"(woff(s % 30) + (CO ^ (s / 30 * 4)) * 65536));
        if (L0 == 32) { xo0 ^= 1536; xo1 ^= 1536; pfw ^= 262144; }
        if (L0 == 64) { xo0 += 1024; xo1 += 1024; }
      }
      if (k == 12 && L0 >= 0) xs[L0 & 31] = ldX(L0 & 31);
      if (k == 28 && L1 >= 0) xs[L1 & 31] = ldX(L1 & 31);
      if ((WM >> k) & 1) ldW(WA + __builtin_popcount(WM & ((1u << k) - 1)));
      if constexpr (k == (J == 0 ? 8 : 30) && FL >= 0) {
        if (G - J + (FL >> 4) * (PS < 2 ? 8 : 7) >= FAS) asm volatile("" :: "a"(F[(PS + (FL >> 4)) & 1][FL & 15]));
        else PIN(F[(PS + (FL >> 4)) & 1][FL & 15]);
      }
      if (k == 30 && (G == PF1 || G == PF2 || G == PF3)) {
        PIN(pf);
        pf = __builtin_amdgcn_raw_buffer_load_b32(rw, pfw, G == PF2 ? 0 : D * D * 2, 0);
        if (XPF) PIN(px);
        if ((XPF & 2) && G < 30) px = __builtin_amdgcn_raw_buffer_load_b32(rx, pfx, (CO ^ 4) * 128 + (G == PF2 ? 1024 : 0), 0);
      }
      if (G == HS && k == 28) {
        hch = (unsigned)(hr >> 32) + 1u;
        __hip_atomic_store(mout, (hr << 32) | hch, RLX_AGENT);
      }
      if (G == 5 && k == 30) PIN(cpf);
      if (G == PF3 + 4 && k == 20) PIN(pf);
      if (G == HG && k == 20) hr = __hip_atomic_load(min_, RLX_AGENT);
      if (G == TS - 2 && k == 26) { ho = (hr << 32) | hch; PIN(ho); }
      if (G == TS - 2 && k == 28) __hip_atomic_store(mout, ho, RLX_AGENT);
      if (G == TS - 2 && k == 12) bi4 = *(const u32x2*)((const char*)bias + po);
      if (G == TS - 2 && k == 16) ga4 = *(const u32x2*)((const char*)gamma + po);
      if (G == TS - 2 && k == 20) be4 = *(const u32x2*)((const char*)beta + po);
      if constexpr (G >= SU) {
        SCHED();
        sfor<SC.v[(G - SU) * 32 + k], SC.v[(G - SU) * 32 + k + 1]>([&](auto UC) {
          constexpr int o = SL.v[decltype(UC)::value + 1];
          if ((o & 31) == 16) PIN(raw[o >> 6][3]);
          else shuf(o >> 6, (o >> 5) & 1, o & 15);
        });
      }
    });
  };
  sfor<1, TS>(step);

  f32x4 p[2][4];
  f32x2 d[4][4], S, Q, pA, pB;
  float piv, pvb[4], z, z2;
  u32x3 q[4];
  unsigned long long hbad;
  float scl[4], shf[4];
  u32x2 o[8];
  auto stA = [&](int a, int i) { *(LDS_AS float*)(lds + aa + i * API + ((a & 7) * 4 + (a >> 3)) * 256) = acc[a & 7][a >> 3][i]; };
  auto rdP = [&](int e, int n) { p[n >> 2][n & 3] = *(const LDS_AS f32x4*)(lds + ((n & 2) ? la1 : la0) + (n & 1) * PREG + (n >> 2) * 4096 + e * 256); };
  auto rdZ = [&](int e) { piv = *(const LDS_AS float*)(lds + lq + e * 256); };
  auto sumP = [&](int e, int h) {
    d[e][2 * h] = (lo2(p[h][0]) + lo2(p[h][1])) + (lo2(p[h][2]) + lo2(p[h][3]));
    d[e][2 * h + 1] = (hi2(p[h][0]) + hi2(p[h][1])) + (hi2(p[h][2]) + hi2(p[h][3]));
  };
  auto stat = [&](int e, int h) {
    f32x2 &d0 = d[e][2 * h], &d1 = d[e][2 * h + 1];
    d0 = fma2(d0, h ? mB : mA, -(h ? pB : pA));
    d1 = fma2(d1, f32x2{mB[1], mB[1]}, -f32x2{pB[1], pB[1]});
    S = h ? (S + d0) + d1 : d0 + d1;
    Q = fma2(d1, d1, h ? fma2(d0, d0, Q) : d0 * d0);
    if (h) { z = S[0] + S[1]; z2 = Q[0] + Q[1]; }
  };
  auto pub = [&](int e, int t) {
    if (t == 0 || t == 6) { auto r = __builtin_amdgcn_permlane32_swap(fu(z), fu(z2), false, false); z = uf(r[0]); z2 = uf(r[1]); }
    if (t == 1) z += z2;
    if (t == 2) z += dpp<0x128>(z);
    if (t == 3) z += dpp<0x124>(z);
    if (t == 4) { auto r = __builtin_amdgcn_permlane16_swap(fu(z), fu(z2), false, false); z = uf(r[0]); z2 = uf(r[1]); }
    if (t == 5) z = uf(min(fu(z + z2), INVALID - 1));
    if (t == 7) __builtin_amdgcn_raw_buffer_store_b96(u32x3{fu(z), fu(pvb[e]), fu(z2)}, rg, gs + e * GST, 0, 16);
    else PIN(z);
  };
  auto ldG = [&](int e) {
    asm volatile("buffer_load_dwordx3 %0, %1, %2, 0 offen offset:%3 sc1" : "=v"(q[e]) : "v"(go), "s"(rg), "n"(e * GST) : "memory");
  };
  auto bad = [&](int e, int n) {
    asm volatile("s_waitcnt vmcnt(%1)" : "+v"(q[e]) : "n"(n) : "memory");
    return __ballot(max(max(q[e][0], q[e][1]), q[e][2]) == INVALID);
  };
  auto poll = [&](int e, int n) {
    if (bad(e, n)) {
#pragma nounroll
      for (int sp = 0; sp < SPIN_CAP; ++sp) {
        __builtin_amdgcn_s_sleep(SLP);
        ldG(e);
        if (!bad(e, 0)) break;
      }
      if (e == 1) { PIN(q[2]); ldG(2); }
    }
  };
  auto coef = [&](int e) {
    const u32x3 v = q[e];
    const float dl = uf(v[1]) - pvb[e];
    float a1 = __builtin_fmaf(nrow, dl, uf(v[0])), a2 = __builtin_fmaf(dl, uf(v[0]) + a1, uf(v[2]));
    sum16(a1, a2);
    const float dm = a1 * (1.0f / (float)L);
    scl[e] = __builtin_amdgcn_rsqf(__builtin_fmaf(-dm, dm, __builtin_fmaf(a2, 1.0f / (float)L, 1e-5f))) * bfel(ga4, e);
    shf[e] = __builtin_fmaf(-dm, scl[e], bfel(be4, e));
  };
  const __amdgpu_buffer_rsrc_t ro = __builtin_amdgcn_make_buffer_rsrc((void*)(out + (size_t)b * L * D), 0, L * D * 2, 0x00020000);
  auto gelu = [&](int e, int i0, int i1) {
#pragma unroll
    for (int i = i0; i < i1; ++i) {
      const f32x2 u = d[e][i] * scl[e] + shf[e];
      const f32x2 t = u * (u * u * -0.1029432397f - 2.3022081985f);
      const f32x2 r = f32x2{__builtin_amdgcn_exp2f(t[0]), __builtin_amdgcn_exp2f(t[1])} + 1.0f;
      d[e][i] = f32x2{__builtin_amdgcn_rcpf(r[0]), __builtin_amdgcn_rcpf(r[1])} * u;
    }
#pragma unroll
    for (int i = 2 * i0; i < 2 * i1; ++i) {
      if (e & 1) o[i][e >> 1] = __builtin_bit_cast(unsigned, bf16x2{(__bf16)d[e - 1][i >> 1][i & 1], (__bf16)d[e][i >> 1][i & 1]});
      if (e == 3) __builtin_amdgcn_raw_buffer_store_b64(o[i], ro, oo, (i >> 2) * 4096 + (i & 3) * 8192, OAUX);
      else if (e == 1) PIN(o[i][0]);
      else PIN(d[e][i >> 1]);
    }
  };
  constexpr int NS = 60 - TS - NX;
  static_assert(TC.v[130] >= 12 * NS && TC.v[194] >= 16 * NS, "");
  auto fin = [&](auto AC) {
    constexpr int a = decltype(AC)::value, e = a >> 3, m = a & 7, r = a < 9 ? 0 : (a - 9) >> 3;
    sfor<0, 8>([&](auto JC) {
      constexpr int j = decltype(JC)::value, s = TS + ((a & 1) ? 7 - j : j), f = a < 9 || (j >= 2 && j <= 5) ? -1 : ((a - 9) & 7) * 4 + (j & 3);
      constexpr int fp = pasof(s) & 1, fi = m + posof(s), u0 = TC.v[8 * a + j], u1 = TC.v[8 * a + j + 1];
      SCHED();
      mfma<(s >= FAS)>(acc[m][e], B[s][e], F[fp][fi]);
      SCHED();
      if constexpr (a == 0 && j < 6) F[1][8 + j] = ldF(3, 8 + j);
      if (a == 1 && j == 7) { if (59 >= FAS) asm volatile("" :: "a"(F[1][13])); else PIN(F[1][13]); }
      if (a > 0 && j >= 2 && j <= 5) stA((a + 31) & 31, j - 2);
      if (f == 1 || (r == 2 && f == 16)) lds_barrier();
      if (f == 2) rdZ(r);
      if (f >= 3 && f <= 10) rdP(r, f - 3);
      if (r == 2 && f == 18) rdZ(3);
      if (r == 2 && (f == 19 || f == 20 || f == 21 || f == 24)) rdP(3, f == 24 ? 3 : f - 19);
      if (a == HA && j == 1) hr = __hip_atomic_load(min_, RLX_AGENT);
      if (a == 24 && j == 7) ldG(0);
      if (f == 26) pub(r, 7);
      SCHED();
      if (a == 24 && j == 6) { hbad = __ballot((unsigned)hr != (unsigned)(ho >> 32)); asm volatile("" :: "s"(hbad)); }
      if (a == 25 && j == 0) { hbad |= __ballot((unsigned)(hr >> 32) != hch); asm volatile("" :: "s"(hbad)); }
      if (f == 14) PIN(p[1][3]);
      if (f == 7 || (r == 2 && f == 24)) PIN(piv);
      if (f == 8 || (r == 2 && f == 26)) { piv += dpp<0x128>(piv); PIN(piv); }
      if (f == 9) { pvb[r] = bfel(bi4, r); PIN(pvb[r]); }
      if (f == 10) { piv += dpp<0x124>(piv); PIN(piv); }
      if (f == 11) { pvb[r] = uf(min(fu(pvb[r] + piv), INVALID - 1)); PIN(pvb[r]); }
      if (r == 2 && f == 20) { pvb[3] = bfel(bi4, 3); PIN(pvb[3]); }
      if (f == 15) {
        pA = mA * piv; pB = mB * piv;
        sumP(r, 0); sumP(r, 1); stat(r, 0); stat(r, 1);
        PIN(z); PIN(z2);
      }
      if (f == 17 || f == 18) pub(r, f - 17);
      if (f >= 21 && f <= 23) pub(r, f - 18);
      if (f == 25) pub(r, 6);
      SCHED();
#pragma unroll
      for (int u = u0; u < u1; ++u) {
        if (u < 12 * NS) shuf(TS + NX + u / 12, 1, u % 12);
        else if (u < 16 * NS) shuf(TS + NX + (u - 12 * NS) / 4, 1, 12 + (u & 3));
      }
      SCHED();
      if (f == 19) pub(r, 2);
    });
  };
  sfor<0, 32>(fin);
  STAMP(2);
  SCHED();
  ldG(1);
  SCHED();
  sumP(3, 0);
  piv += dpp<0x124>(piv);
  PIN(piv); PIN(d[3][0]); PIN(d[3][1]);
  SCHED();
#pragma unroll
  for (int i = 0; i < 4; ++i) { stA(31, i); SCHED(); }
  pA = mA * piv; pB = mB * piv;
  stat(3, 0);
  pvb[3] = uf(min(fu(pvb[3] + piv), INVALID - 1));
  hbad |= bad(0, 2);
  asm volatile("" :: "s"(hbad), "v"(pvb[3]));
  PIN(S); PIN(Q);
  SCHED();
  lds_barrier();
#pragma unroll
  for (int i = 4; i < 8; ++i) rdP(3, i);
  SCHED();
  coef(0);
  PIN(scl[0]); PIN(shf[0]);
  SCHED();
  sumP(3, 1); stat(3, 1);
  STAMP(3);
#pragma unroll
  for (int t = 0; t < 8; ++t) pub(3, t);
  SCHED();
  STAMP(7);
  if (__builtin_expect(hbad != 0, 0)) {
    unsigned sent = (unsigned)(ho >> 32);
    asm volatile("s_waitcnt vmcnt(0)" :: "v"(q[1]) : "memory");
#pragma nounroll
    for (int sp = 0; sp < SPIN_CAP; ++sp) {
      if (__any((unsigned)hr != sent)) { __hip_atomic_store(mout, (hr << 32) | hch, RLX_AGENT); sent = (unsigned)hr; }
      if (__all((unsigned)(hr >> 32) == hch)) break;
      __builtin_amdgcn_s_sleep(1);
      hr = __hip_atomic_load(min_, RLX_AGENT);
    }
    asm volatile("s_waitcnt vmcnt(0)" :: "v"(hr) : "memory");
    ldG(0); ldG(1);
    poll(0, 0);
    coef(0);
    asm volatile(".Lconv_code_end:" :: "v"(scl[0]), "v"(shf[0]));
  }
  gelu(0, 0, 4);
  SCHED();
  ldG(2);
  XST(0);
  poll(1, 2);
  XST(1);
  coef(1);
  gelu(1, 0, 4);
  SCHED();
  XST(2);
  poll(2, 0);
  ldG(3);
  XST(3);
  SCHED();
  coef(2);
  gelu(2, 0, 4);
  XST(4);
  poll(3, 0);
  XST(5);
  coef(3);
  STAMP(4);
  gelu(3, 0, 2);
  SCHED();
  gelu(3, 2, 4);
  STAMP(5);
}

extern "C" void kernel_launch(void* const* d_in, const int* in_sizes, int n_in,
                              void* d_out, int out_size, void* d_ws, size_t ws_size,
                              hipStream_t stream) {
  const __bf16* x = (const __bf16*)d_in[0];
  const __bf16* w = (const __bf16*)d_in[1];
  const __bf16* bias = (const __bf16*)d_in[2];
  const __bf16* gamma = (const __bf16*)d_in[3];
  const __bf16* beta = (const __bf16*)d_in[4];
  unsigned long long* gran = (unsigned long long*)d_ws;
  unsigned long long* stamps = (unsigned long long*)((char*)d_ws + (size_t)32 * 1024 * 1024);
  conv_gn_gelu_kernel<<<256, 256, 0, stream>>>(x, w, bias, gamma, beta, (__bf16*)d_out, gran, stamps);
}
```

```cpp
#include <hip/hip_runtime.h>
#include <type_traits>
#include <utility>

typedef __bf16 bf16x2 __attribute__((ext_vector_type(2)));
typedef __bf16 bf16x4 __attribute__((ext_vector_type(4)));
typedef __bf16 bf16x8 __attribute__((ext_vector_type(8)));
typedef float f32x4 __attribute__((ext_vector_type(4)));
typedef float f32x2 __attribute__((ext_vector_type(2)));
typedef unsigned u32x2 __attribute__((ext_vector_type(2)));
typedef unsigned u32x3 __attribute__((ext_vector_type(3)));
typedef unsigned u32x4 __attribute__((ext_vector_type(4)));
#define LDS_AS __attribute__((address_space(3)))
#define GAS __attribute__((address_space(1)))
#define DI __device__ __forceinline__

constexpr int T = 4000, D = 512, KT = 15, L = 1993;
constexpr int XPL = 544, XRES = 4 * XPL, XIMG = 8 * XRES, WREG = 2 * XIMG;
constexpr int LDS_BYTES = 4 * WREG;
constexpr int API = 8256, PREG = WREG + 16;
constexpr int SPIN_CAP = 1 << 20;
constexpr unsigned INVALID = 0xFFFFFFFFu;
constexpr int GST = 128;
constexpr int GRAN_BYTES = 4096 * GST;

#define RLX_AGENT __ATOMIC_RELAXED, __HIP_MEMORY_SCOPE_AGENT
#define SCHED() __builtin_amdgcn_sched_barrier(0)
#define FAS 46
#define PF1 6
#define PF2 20
#define PF3 36
#define GB 43
#define RB 4
#define SLP 1
#define HG 46
#define HA 12
#define OAUX 0
#define XPF 0
#define MAP 2
#define PFM 0
#define PD 5
#define PG 2
#define NTX 0
#define PF0 0
#define PFN 16
#define TOP 1
#define SHT 0
#define PFS 1
#define BX 0
#define WP 0
#define WQ 1
#define CP0 2
#define PFH 0
#define PEN 9
#define NX 0
constexpr int TS = 52;
constexpr int SU = 42;
#define RING 3
#define NPRE 8
#define NQ 3
#define HS 11
#define HD 4
#define XD 3
#define CO 4
#define QW (PFM ? 0 : 1)
constexpr unsigned woff(int S) {
  return S < 16 ? (unsigned)((2 * (S & 7)) * (D * D * 2) + (S >> 3) * 32768)
                : (unsigned)((2 * ((S - 16) % 7) + 1) * (D * D * 2) + ((S - 16) / 7) * 32768);
}
constexpr int pasof(int G) { return G % 30 < 8 ? 0 : G % 30 < 16 ? 1 : G % 30 < 23 ? 2 : 3; }
constexpr int posof(int G) { return G % 30 - (pasof(G) == 0 ? 0 : pasof(G) == 1 ? 8 : pasof(G) == 2 ? 16 : 23); }
constexpr bool quiet(int G) { return (G > PF1 && G <= PF1 + QW) || (G > PF2 && G <= PF2 + QW) || (G > PF3 && G <= PF3 + QW); }
constexpr int xst(int im, int p) {
  const int a = im == 0 ? XD : im == 1 ? 16 : 30, z = im == 0 ? 15 : im == 1 ? 29 : 45;
  int n = 0;
  for (int g = a; g <= z; ++g) n += !quiet(g - XD);
  int i = (p * (n - 1) + 8) / 16;
  for (int g = a; g <= z; ++g)
    if (!quiet(g - XD) && i-- == 0) return g;
  return -1;
}
constexpr int xq(int G, int n) {
  for (int im = 0; im < 3; ++im)
    for (int p = 0; p < 17; ++p)
      if (xst(im, p) == G && n-- == 0) return im * 32 + p;
  return -1;
}
constexpr int wcnt(int G) {
  int n = 4 * (G + RING + 1) + (G > GB ? RB * (G - GB) : 0);
  for (int i = 0; i < 3; ++i) {
    const int d = G - 1 - (i == 0 ? PF1 : i == 1 ? PF2 : PF3);
    n += d <= 0 ? (4 * QW + d > 0 ? 4 * QW + d : 0) : (4 * QW - 4 * d > 0 ? 4 * QW - 4 * d : 0);
  }
  return n < 240 ? n : 240;
}
constexpr int qdone(int q) {
  int n = 0;
  for (int i = 0; i < q; ++i) {
    if (2 * i == WP) n += WQ < i ? WQ : i;
    n += 2 * i < WP ? 0 : i == 8 ? 8 - n : i - n < 2 ? i - n : 2;
  }
  return n;
}
constexpr int pbs(int k) { return k / 8 * 30 + (k % 8 < 6 ? 4 * (k % 8) : 4 * (k % 8) - 1); }
constexpr int pslot(int k) {
  int sl = SU + PG - 2;
  for (int i = 15; i >= k; --i) {
    int G = 0;
    while (wcnt(G + 1) <= 4 * pbs(i)) ++G;
    sl = G - PD < sl - PG ? G - PD : sl - PG;
  }
  return sl < 0 ? 0 : sl;
}
constexpr int pblk(int G) {
  for (int k = 1; k < 16; ++k)
    if (pslot(k) == G) return k;
  return -1;
}
constexpr unsigned wmask(int G) {
  int wn = wcnt(G + 1) - wcnt(G);
  const int pos[8] = {14, 18, 22, 26, xq(G + XD, 0) < 0 ? 12 : -1, xq(G + XD, 1) < 0 ? 28 : -1, 30, 24};
  unsigned m = 0;
  for (int i = 0; i < 8; ++i)
    if (pos[i] >= 0 && wn > 0) { m |= 1u << pos[i]; --wn; }
  return m;
}
constexpr int fq(int G, int k) {
  const int PS = pasof(G), J = posof(G), NT = PS < 2 ? 8 : 7, JN = J - NT + 4 + (PS == 2);
  if (G == 1) return k == 0 ? 9 : k == 2 ? 10 : -1;
  if (J == 0 && k == 0) return 8;
  if (k == (J == 0 ? 2 : 0) && J + 2 < NT) return 9 + J;
  if ((k == 2 || k == 4) && JN >= 0 && JN < 4) return 16 + 2 * JN + (k == 4);
  return -1;
}
struct Tab { int v[512]; };
constexpr Tab slist() {
  Tab t{};
  int n = 0;
  for (int h = 0; h < 2 * (TS - SU) + NX + 58 - TS; ++h) {
    const int x = h - 2 * (TS - SU), G = SU + (h >> 1);
    const int s = x < 0 ? ((G + 1) & ~1) + (h & 1) : x < NX ? TS + x : TS + 2 + x - NX, j = x < 0 ? (G + 1) & 1 : x < NX;
    if (!j) t.v[++n] = s * 64 + 16;
    for (int i = 0; i < 16; ++i) t.v[++n] = s * 64 + j * 32 + i;
  }
  t.v[0] = n;
  return t;
}
constexpr Tab SL = slist();
constexpr Tab scum() {
  Tab t{};
  int n = SL.v[0];
  t.v[(TS - SU) * 32] = n;
  for (int G = TS - 1; G >= SU; --G) {
    const unsigned wm = wmask(G) | (xq(G, 0) >= 0 ? 0x154u : 0u) | (xq(G, 1) >= 0 ? 0x1110000u : 0u) | (wmask(G) ? 0x400u : 0u) |
                        (G == TS - 2 ? 0x14111000u : 0u) | (G == HG ? 1u << 20 : 0u) | (xq(G + XD, 0) >= 0 ? 1u << 12 : 0u) | (xq(G + XD, 1) >= 0 ? 1u << 28 : 0u);
    for (int k = 31; k >= 0; --k) {
      n -= (k & 1) ? 2 : ((wm >> k) & 1) ? 0 : 2 - (fq(G, k) >= 0) - (k == (posof(G) == 0 ? 8 : 30));
      const int o = (G - SU) * 33 - (G & 1) + (k & ~1) + ((G & 1) ? (k > 0) + (k > 16) : 0);
      if (n < o) n = o;
      t.v[(G - SU) * 32 + k] = n;
    }
  }
  return t;
}
constexpr int flast(int G) { return fq(G, 4) >= 0 ? fq(G, 4) : fq(G, 2) >= 0 ? fq(G, 2) : fq(G, 0); }
constexpr int tocc(int a, int j) {
  if (a == 0) return j < 6;
  if (j >= 2 && j <= 5) return 2;
  if (a < 9) return a == 1 && j == 7;
  if (a == HA && j == 1) return 2;
  const int occ[32] = {0, 2, 1, 1, 1, 1, 1, 2, 2, 2, 2, 2, 0, 0, 1, 2, 0, 2, 1, 1, 0, 1, 2, 2, 0, 2, 1};
  return occ[((a - 9) & 7) * 4 + (j & 3)];
}
constexpr Tab tcum() {
  Tab t{};
  for (int u = 0; u < 256; ++u) t.v[u + 1] = t.v[u] + 2 - tocc(u >> 3, u & 7);
  return t;
}
constexpr Tab SC = scum(), TC = tcum();
static_assert(SC.v[0] == 0 && !(SU & 1), "");
template <int A, int... I, class FT> DI void sfor_(std::integer_sequence<int, I...>, FT&& f) { (f(std::integral_constant<int, A + I>{}), ...); }
template <int A, int Z, class FT> DI void sfor(FT&& f) { sfor_<A>(std::make_integer_sequence<int, Z - A>{}, f); }

DI unsigned xsel(unsigned oth, unsigned own) {
  unsigned r;
  register unsigned long long m asm("vcc") = 0xFF00FF00FF00FF00ull;
  asm volatile("v_cndmask_b32_dpp %0, %1, %2, vcc row_ror:8 row_mask:0xf bank_mask:0xf" : "=v"(r) : "v"(oth), "v"(own), "s"(m));
  return r;
}
template <bool FA>
DI void mfma(f32x4& acc, const u32x4& a, const bf16x8& b) {
  if (FA) asm volatile("v_mfma_f32_16x16x32_bf16 %0, %1, %2, %0" : "+a"(acc) : "v"(a), "a"(b));
  else asm volatile("v_mfma_f32_16x16x32_bf16 %0, %1, %2, %0" : "+a"(acc) : "v"(a), "v"(b));
}
DI void mfma0(f32x4& acc, const u32x4& a, const bf16x8& b) {
  asm volatile("v_mfma_f32_16x16x32_bf16 %0, %1, %2, 0" : "=a"(acc) : "v"(a), "v"(b));
}
DI void setm0(unsigned v) { asm volatile("s_mov_b32 m0, %0" :: "s"(v) : "memory"); }
DI void st_tid(unsigned v, int off) { asm volatile("ds_write_addtid_b32 %0 offset:%1" :: "v"(v), "n"(off) : "memory"); }
DI void lds_barrier() {
  asm volatile("" ::: "memory");
  __builtin_amdgcn_s_waitcnt(0xC07F);
  __builtin_amdgcn_s_barrier();
  asm volatile("" ::: "memory");
}
DI unsigned fu(float v) { return __builtin_bit_cast(unsigned, v); }
DI float uf(unsigned v) { return __builtin_bit_cast(float, v); }
DI f32x2 lo2(const f32x4& v) { return f32x2{v[0], v[1]}; }
DI f32x2 hi2(const f32x4& v) { return f32x2{v[2], v[3]}; }
DI f32x2 fma2(f32x2 a, f32x2 b, f32x2 c) { return __builtin_elementwise_fma(a, b, c); }
DI float bfel(const u32x2& v, int e) { return uf((e & 1) ? v[e >> 1] & 0xFFFF0000u : v[e >> 1] << 16); }
#define PIN(x) asm volatile("" :: "v"(x))
template <int CTRL>
DI float dpp(float v) { return __builtin_bit_cast(float, __builtin_amdgcn_update_dpp(0, __builtin_bit_cast(int, v), CTRL, 0xF, 0xF, true)); }

DI void sum16(float& a, float& b) {
  auto p = __builtin_amdgcn_permlane32_swap(fu(a), fu(b), false, false);
  float v = uf(p[0]) + uf(p[1]);
  v += dpp<0x128>(v);
  v += dpp<0x124>(v);
  auto r = __builtin_amdgcn_permlane16_swap(fu(v), fu(v), false, false);
  v = uf(r[0]) + uf(r[1]);
  auto q = __builtin_amdgcn_permlane32_swap(fu(v), fu(v), false, false);
  a = uf(q[0]); b = uf(q[1]);
}

#define SST(k) do {} while (0)
#define XST(k) do {} while (0)
#define STAMP(k) do {} while (0)

__global__ __launch_bounds__(256, 1)
void conv_gn_gelu_kernel(const void* w, unsigned wn, unsigned wf, const void* x0, unsigned x0n, unsigned x0f, const void* x1, unsigned x1n, unsigned x1f,
                         void* o0, unsigned o0n, unsigned o0f, void* o1, unsigned o1n, unsigned o1f, unsigned long long* gran, unsigned gn, unsigned gf,
                         const void* bi, const void* ga, const void* be,
                         unsigned long long* __restrict__ stamps) {
  typedef const __amdgpu_buffer_rsrc_t __attribute__((address_space(4)))* KA;
  const KA ka = (KA)__builtin_amdgcn_kernarg_segment_ptr();
  const __amdgpu_buffer_rsrc_t rw = ka[0], rx = ka[1 + ((blockIdx.x >> (MAP ? 1 : 0)) & 1)];
  SCHED();
  __shared__ __attribute__((aligned(1024))) char smem[LDS_BYTES];
  LDS_AS char* const lds = (LDS_AS char*)smem;
  const int tid = threadIdx.x, lane = tid & 63;
  const int wv = __builtin_amdgcn_readfirstlane(tid >> 6);
  const int bid = blockIdx.x;
  const int xcd = bid & 7, idx = bid >> 3;
  const int mt = ((xcd >> (MAP ? 1 : 0)) & 1) * 16 + (idx & 15), nt = (MAP ? (xcd & 1) * 4 + (xcd >> 2) * 2 : xcd & 6) + (idx >> 4);
  const int b = mt >> 4, sh = (SHT >> 4 * nt) & 7, l0 = (mt & 15) * 128 + sh, n0 = (nt ^ NTX) * 64;
  const int c = lane & 15, g = lane >> 4;
  STAMP(0);

  auto ldC = [&](int skip) {
    unsigned long long pc;
    unsigned len;
    asm volatile("s_getpc_b64 %0\n\ts_mov_b32 %1, .Lconv_code_end-." : "=s"(pc), "=s"(len));
    const __amdgpu_buffer_rsrc_t rc = __builtin_amdgcn_make_buffer_rsrc((void*)pc, 0, len, 0x00020000);
    return __builtin_amdgcn_raw_buffer_load_b32(rc, (unsigned)((lane * 128 + idx * 4 + wv) * 128) | (skip == 1 && lane ? 0x80000000u : 0u), skip == 2 ? 12288 : 0, 0);
  };
  unsigned cpf, cp0 = 0;
  if (CP0 == 1) cpf = ldC(0);
  if (CP0 == 2) cp0 = ldC(1);
  SCHED();
  unsigned wo = (unsigned)((2 * g + 16 * (c >> 3)) * 1024 + n0 * 2 + (c & 7) * 16 + wv * 65536), wo2 = wo + 8192;
  unsigned pfe = PFH == 2 ? (unsigned)(((lane >> 2) * 2 * D + (lane & 2) * 16 + idx) * (D * 2) + (n0 & ~64) * 2 + (lane & 1) * 128 + (wv + CO) * 65536)
                          : (unsigned)(((lane >> 1) * 2 * D + idx) * (D * 2) + (n0 & ~64) * 2 + (lane & 1) * 128 + (wv + CO) * 65536);
  const int pes = WP || PFH == 2 ? 0 : 2 * D * D * 2;
  unsigned xo0 = (unsigned)((2 * l0 + 16 * (lane >> 3)) * (D * 2) + ((lane ^ (lane >> 1)) & 4) * 16 + (lane & 3) * 16 + (wv + CO) * 128);
  const bool last = (mt & 15) == 15;
  unsigned xoh = xo0 + ((last && (lane >> 4) == 2) ? (unsigned)(-(2 * l0 + 64) * (D * 2)) : 131072u);
  if (TOP) { asm volatile("" : "+v"(wo), "+v"(wo2), "+v"(pfe), "+v"(xo0), "+v"(xoh)); SCHED(); }

  const int grp = b * 8 + nt;
  const int gbase = (grp * 4 + wv) * 64 * GST;
  __amdgpu_buffer_rsrc_t rg, ro;
  GAS unsigned long long *min_, *mout;
  const char *bias, *gamma, *beta;

  u32x4 raw[60][4];
  unsigned so[60];
  auto ldW = [&](int n) {
    raw[n >> 2][n & 3] = __builtin_bit_cast(u32x4, __builtin_amdgcn_raw_buffer_load_b128(rw, ((n & 2) ? wo2 : wo) + (n & 1) * 1024, so[n >> 2], 0));
  };
#pragma unroll
  for (int i = 0; i < RING + 2; ++i) so[i] = woff(i) + CO * 65536;
  unsigned pe = 0;
  if (PF0 == 1) pe = __builtin_amdgcn_raw_buffer_load_b32(rw, pfe, pes, 0);
  SCHED();
#pragma unroll
  for (int i = 0; i < 4; ++i) if (!WP) ldW(i);
  SCHED();
  if (PF0 == 2) pe = __builtin_amdgcn_raw_buffer_load_b32(rw, pfe, pes, 0);
  SCHED();
  SST(0);

  unsigned xo1 = (unsigned)((2 * l0 + 256 + 2 * (lane >> 3)) * (D * 2) + (lane & 7) * 16 + (wv + CO) * 128);
  const unsigned xa1 = (unsigned)(wv * WREG + (lane >> 3) * XRES + 512 + (lane & 7) * 4);
  auto ldX = [&](int pc) -> u32x4 {
    return __builtin_bit_cast(u32x4, __builtin_amdgcn_raw_buffer_load_b128(rx, pc == 16 ? xo1 : (pc & 1) ? xoh : xo0, (pc & 14) * 1024, 0));
  };
  auto stX = [&](int p, int pc, const u32x4& v, int d) {
    if (pc == 16) *(LDS_AS unsigned*)(lds + xa1 + p * XIMG + d * XPL) = v[d];
    else st_tid(v[d], p * XIMG + (pc >> 1) * XRES + d * XPL + (pc & 1) * 256);
  };
  u32x4 xs[32];
  unsigned long long hr;
  auto inval = [&]() { __builtin_amdgcn_raw_buffer_store_b64(u32x2{INVALID, INVALID}, rg, lane * 8, gbase + (mt & 15) * 4 * GST, 16); };
  auto ldS = [&](int n) {
    if (n < 16) {
      xs[n & 15] = ldX(n);
      if (n == WP - 1) { SCHED(); ldW(0); ldW(1); ldW(2); ldW(3); }
    }
    else if (n < 20) ldW(n - 12);
    else if (n == 20) xs[16] = ldX(16);
    else if (n < 21 + 4 * (RING - 1)) {
      ldW(n - 13);
      if (n == 20 + 4 * (HS - 1)) { inval(); hr = __hip_atomic_load(min_, RLX_AGENT); }
    }
    if (PF0 == 4 && n == PEN) { SCHED(); pe = __builtin_amdgcn_raw_buffer_load_b32(rw, pfe, pes, 0); }
  };
#pragma unroll
  for (int i = 0; i < NPRE; ++i) { ldS(i); SCHED(); }
  if (PF0 == 3) pe = __builtin_amdgcn_raw_buffer_load_b32(rw, pfe, pes, 0);
  SCHED();
  typedef char* const __attribute__((address_space(4)))* KP;
  rg = ka[5]; ro = ka[3 + b];
  bias = ((KP)ka)[12]; gamma = ((KP)ka)[13]; beta = ((KP)ka)[14];
  GAS unsigned long long* mbase = (GAS unsigned long long*)((KP)ka)[10];
  SCHED();
  setm0(wv * WREG);

  unsigned pfr[16], pso;
  unsigned pfw = PFS ? (unsigned)(((lane >> 2) * 2 * D + idx * 2 + ((lane >> 1) & 1)) * (D * 2) + (n0 & ~64) * 2 + (lane & 1) * 128 + (wv + CO) * 65536)
                     : (unsigned)(((lane >> 2) * 2 * D + (mt & 15) * 4 + (lane & 3)) * (D * 2) + n0 * 2 + (wv + CO) * 65536);

  const unsigned pfx = lane < 16 ? (unsigned)((2 * l0 + 32 * nt + 2 * lane) * (D * 2) + wv * 128) : 0x80000000u;
  unsigned px = 0;
  const int cb = n0 + 32 * (wv & 1) + 4 * (wv >> 1), ei = lane & 3, cq = c >> 2;
  unsigned aa = (unsigned)(wv * PREG + lane * 4);
  unsigned la0 = (unsigned)(ei * API + wv * 64 + g * 1024 + cq * 16), la1 = la0 + 2 * PREG;
  unsigned lq = (unsigned)(ei * API + wv * 64 + cq * PREG + (ei >> 1) * 4);
  unsigned gs = lane >= 4 ? 0x80000000u : (unsigned)(gbase + (mt & 15) * 4 * GST + ei * 16);
  unsigned go = (unsigned)(gbase + (lane >> 2) * 4 * GST + ei * 16);
  unsigned oo = (unsigned)((l0 + 32 * cq + g) * (D * 2) + (cb + 8 * ei) * 2);
  const int lim = (!last || cq < 2 ? 99 : cq == 2 ? 9 - sh : sh) - g;
  const unsigned ow = last && cq == 3 ? (unsigned)((l0 + 96) * (D * 2)) : 0u;
  unsigned oz = oo - (lim > 0 ? ow : 0u), oy = oo - (lim > 4 ? ow : 0u);
  f32x2 mA = {lim > 0 ? 1.f : 0.f, lim > 8 ? 1.f : 0.f}, mB = {lim > 4 ? 1.f : 0.f, lim > 12 ? 1.f : 0.f};
  unsigned po = (unsigned)((cb + 8 * ei) * 2);
  float nrow = lane >= 60 ? (float)(L - 15 * 128) : 128.f;
  asm volatile("" : "+v"(po), "+v"(nrow), "+v"(aa), "+v"(la0), "+v"(la1), "+v"(lq), "+v"(gs), "+v"(go), "+v"(oo), "+v"(oz), "+v"(oy), "+v"(mA), "+v"(mB));

  SCHED();
  mbase += GRAN_BYTES / 8 + (grp * 4 + wv) * 256;
  min_ = mbase + (mt & 15) * 16 + c;
  mout = mbase + c * 16 + (mt & 15);
  asm volatile("" : "+s"(ro), "+s"(bias), "+s"(gamma), "+s"(beta));

  unsigned fa[2][2];
#pragma unroll
  for (int hi = 0; hi < 2; ++hi) {
    const int v = c + hi;
    fa[0][hi] = (unsigned)(wv * WREG + g * XPL + v * 32 + (v & 1) * 16);
    fa[1][hi] = fa[0][hi] ^ 16u;
  }
  auto ldF = [&](int pass, int q) -> bf16x8 {
    return *(LDS_AS bf16x8*)(lds + fa[pass & 1][q >> 3] + (pass >> 1) * XIMG + (q & 7) * XRES);
  };
  unsigned U[60][4][2], V[60][4][2];
  u32x4 B[60][4];
  auto mkU = [&](int s, int r, int j) { U[s][r][j] = xsel(raw[s][r][j], raw[s][r][2 + j]); };
  auto mkV = [&](int s, int r, int j) { V[s][r][j] = __builtin_amdgcn_update_dpp(raw[s][r][j], raw[s][r][2 + j], 0x128, 0xF, 0xC, false); };
  auto mkB = [&](int s, int e, int d) {
    const unsigned sel = (e & 1) ? 0x07060302u : 0x05040100u;
    B[s][e][d] = d < 2 ? __builtin_amdgcn_perm(V[s][2 * d + 1][e >> 1], V[s][2 * d][e >> 1], sel)
                       : __builtin_amdgcn_perm(U[s][2 * d - 3][e >> 1], U[s][2 * d - 4][e >> 1], sel);
  };

  f32x4 acc[8][4];
  bf16x8 F[2][15];
  sfor<0, 9>([&](auto QC) {
    constexpr int q = decltype(QC)::value, q0 = qdone(q), q1 = qdone(q + 1), qa = 2 * q == WP ? (WQ < q ? WQ : q) : 0;
    if (q == 0) SST(1);
    if (2 * q == WP) {
      SCHED();
#pragma unroll
      for (int i = 0; i < 8; ++i) mkU(0, i >> 1, i & 1);
      SCHED();
      SST(2);
#pragma unroll
      for (int i = 0; i < 8; ++i) mkV(0, i >> 1, i & 1);
#pragma unroll
      for (int i = 0; i < 16; ++i) mkB(0, i >> 2, i & 3);
      SCHED();
#pragma unroll
      for (int i = 0; i < 4 * qa; ++i) mfma0(acc[i >> 2][i & 3], B[0][i & 3], F[0][i >> 2]);
      SCHED();
    }
    if (q < 8) {
#pragma unroll
      for (int i = 0; i < 8; ++i) stX(0, 2 * q + (i >> 2), xs[2 * q + (i >> 2)], i & 3);
      F[0][q] = ldF(0, q);
      if (q == 0) SST(3);
      if (q == 3) SST(4);
      if (q == 7) SST(5);
    }
#pragma unroll
    for (int e = 0; e < (q1 - q0 - qa > 1 ? 4 * (q1 - q0 - qa) : 4); ++e) {
      SCHED();
      if (q0 + qa + (e >> 2) < q1) mfma0(acc[q0 + qa + (e >> 2)][e & 3], B[0][e & 3], F[0][q0 + qa + (e >> 2)]);
      SCHED();
      if (e < NQ) ldS(NPRE + NQ * q + e);
    }
  });
  static_assert(NPRE + NQ * 9 >= 21 + 4 * (RING - 1), "");
  xo0 += 1024; xoh += 1024; xo1 += 1024;
  SCHED();
#pragma unroll
  for (int i = 0; i < 8; ++i) mkU(1, i >> 1, i & 1);
  SCHED();
  SST(6);
#pragma unroll
  for (int i = 0; i < 8; ++i) mkV(1, i >> 1, i & 1);
#pragma unroll
  for (int i = 0; i < 4; ++i) stX(0, 16, xs[16], i);
  F[0][8] = ldF(0, 8);
#pragma unroll
  for (int i = 0; i < 16; ++i) mkB(1, i >> 2, i & 3);
  if (PF0 && PEN < 20) PIN(pe);
  if (CP0 == 2) PIN(cp0);
  SCHED();
#pragma unroll
  for (int i = 0; i < 4; ++i) ldW(wcnt(0) + i);
  xs[0] = ldX(0);
  if (xq(XD, 1) >= 0) xs[1] = ldX(1);
  unsigned pf = 0;
  if (PFH == 0) pf = __builtin_amdgcn_raw_buffer_load_b32(rw, pfw, 0, 0);
  if (PFH == 1) pf = __builtin_amdgcn_raw_buffer_load_b32(rw, pfe, 32768, 0);
#pragma unroll
  for (int i = 1; i < 16; ++i)
    if (PFM && pslot(i) == 0) pfr[i] = __builtin_amdgcn_raw_buffer_load_b32(rw, pfw, woff(pbs(i)) + CO * 65536, 0);
  if (XPF & 1) px = __builtin_amdgcn_raw_buffer_load_b32(rx, pfx, CO * 128 + 1024, 0);
  if (CP0 != 1) cpf = ldC(CP0 ? 2 : 0);
  u32x2 bi4, ga4, be4;
  SCHED();
  STAMP(1);
  unsigned hch;
  unsigned long long ho;

  auto shuf = [&](int s, int j, int t) {
    if (t >= 8) mkB(s, 2 * j + ((t >> 2) & 1), t & 3);
    else if (t & 1) mkV(s, t >> 1, j);
    else mkU(s, t >> 1, j);
  };
  auto step = [&](auto GC) {
    constexpr int G = decltype(GC)::value;
    constexpr int PS = pasof(G), J = posof(G);
    constexpr int W0 = xq(G, 0), W1 = xq(G, 1), L0 = xq(G + XD, 0), L1 = xq(G + XD, 1);
    constexpr int WA = wcnt(G), WN = wcnt(G + 1) - WA;
    constexpr unsigned WM = wmask(G);
    constexpr int PBK = pblk(G);
    constexpr int G0 = G & ~1, N0 = (G + 1) & ~1, NJ = (G + 1) & 1;
    static_assert(xq(G, 2) < 0 && WN >= 0 && WN == __builtin_popcount(WM), "");
    static_assert(W0 < 0 || G < ((W0 >> 5) == 0 ? 12 : (W0 >> 5) == 1 ? 26 : 42) + ((W0 & 31) >> 2), "");
    static_assert(W1 < 0 || G < ((W1 >> 5) == 0 ? 12 : (W1 >> 5) == 1 ? 26 : 42) + ((W1 & 31) >> 2), "");
    static_assert(wcnt(TS - 1) == 240, "");

    sfor<0, 32>([&](auto KC) {
      constexpr int k = decltype(KC)::value, FQ = fq(G, k), FL = flast(G);
      constexpr int e = G == 1 ? k >> 3 : 2 * (G & 1) + (k >> 4), kk = (k & 16) ? 31 - k : k;
      constexpr int m = G == 1 ? k & 7 : kk >> 1, sj = G == 1 ? 1 : G0 + (kk & 1), fp = pasof(sj) & 1, fi = m + posof(sj);
      SCHED();
      mfma<(sj >= FAS)>(acc[m][e], B[sj][e], F[fp][fi]);
      SCHED();
      if ((k & 1) && G < SU) { shuf(N0 + (k >> 4), NJ, (k - 1) & 15); shuf(N0 + (k >> 4), NJ, k & 15); }
      if constexpr (FQ >= 0) F[(PS + (FQ >> 4)) & 1][FQ & 15] = ldF((PS + (FQ >> 4)) & 3, FQ & 15);
      if ((k == 0 || k == 16) && NJ == 0 && G < SU) PIN(raw[N0 + (k >> 4)][3]);
      if (k >= 2 && k <= 8 && !(k & 1) && W0 >= 0) stX((W0 >> 5) != 1, W0 & 31, xs[W0 & 31], (k - 2) >> 1);
      if ((k == 10 || k == 16 || k == 20 || k == 24) && W1 >= 0) stX((W1 >> 5) != 1, W1 & 31, xs[W1 & 31], k == 10 ? 0 : (k - 12) >> 2);
      if (k == 10) {
#pragma unroll
        for (int s = (WA + 3) >> 2; 4 * s < WA + WN; ++s) asm volatile("s_mov_b32 %0, %1" : "=s"(so[s]) : "n"(woff(s % 30) + (CO ^ (s / 30 * 4)) * 65536));
        if (L0 == 32) { xo0 ^= 1536; xoh ^= 1536; xo1 ^= 1536; if (!PFM) pfw ^= 262144; }
        if (L0 == 64) { xo0 += 1024; xoh += 1024; xo1 += 1024; }
      }
      if (k == 12 && L0 >= 0) xs[L0 & 31] = ldX(L0 & 31);
      if (k == 28 && L1 >= 0) xs[L1 & 31] = ldX(L1 & 31);
      if ((WM >> k) & 1) ldW(WA + __builtin_popcount(WM & ((1u << k) - 1)));
      if constexpr (k == (J == 0 ? 8 : 30) && FL >= 0) {
        if (G - J + (FL >> 4) * (PS < 2 ? 8 : 7) >= FAS) asm volatile("" :: "a"(F[(PS + (FL >> 4)) & 1][FL & 15]));
        else PIN(F[(PS + (FL >> 4)) & 1][FL & 15]);
      }
      if constexpr (PFM && PBK > 0) {
        if (k == 6) asm volatile("s_mov_b32 %0, %1" : "=s"(pso) : "n"(woff(pbs(PBK) % 30) + (CO ^ (PBK / 8 * 4)) * 65536));
        if (k == (L1 < 0 && G != HS ? 28 : W1 < 0 ? 24 : 30)) pfr[PBK] = __builtin_amdgcn_raw_buffer_load_b32(rw, pfw, pso, 0);
      }
      if constexpr (PFM && k == 20) {
#pragma unroll
        for (int i = 1; i < 16; ++i)
          if (pslot(i) + 5 == G) PIN(pfr[i]);
      }
      if (!PFM && k == 30 && (G == PF1 || G == PF2 || G == PF3)) {
        PIN(pf);
        pf = __builtin_amdgcn_raw_buffer_load_b32(rw, pfw, G == PF2 ? 0 : D * D * 2, 0);
        if (XPF) PIN(px);
        if ((XPF & 2) && G < 30) px = __builtin_amdgcn_raw_buffer_load_b32(rx, pfx, (CO ^ 4) * 128 + (G == PF2 ? 1024 : 0), 0);
      }
      if (HS > RING && G == HS - HD && k == 12) inval();
      if (HS > RING && G == HS - HD && k == 14) hr = __hip_atomic_load(min_, RLX_AGENT);
      if (G == HS && k == 28) {
        hch = (unsigned)(hr >> 32) + 1u;
        __hip_atomic_store(mout, (hr << 32) | hch, RLX_AGENT);
      }
      if (G == 5 && k == 30) { PIN(cpf); PIN(pe); }
      if (!PFM && G == PF3 + 4 && k == 20) PIN(pf);
      if (G == HG && k == 20) hr = __hip_atomic_load(min_, RLX_AGENT);
      if (G == TS - 2 && k == 26) { ho = (hr << 32) | hch; PIN(ho); }
      if (G == TS - 2 && k == 28) __hip_atomic_store(mout, ho, RLX_AGENT);
      if (G == TS - 2 && k == 12) bi4 = *(const GAS u32x2*)(bias + po);
      if (G == TS - 2 && k == 16) ga4 = *(const GAS u32x2*)(gamma + po);
      if (G == TS - 2 && k == 20) be4 = *(const GAS u32x2*)(beta + po);
      if constexpr (G >= SU) {
        SCHED();
        sfor<SC.v[(G - SU) * 32 + k], SC.v[(G - SU) * 32 + k + 1]>([&](auto UC) {
          constexpr int o = SL.v[decltype(UC)::value + 1];
          if ((o & 31) == 16) PIN(raw[o >> 6][3]);
          else shuf(o >> 6, (o >> 5) & 1, o & 15);
        });
      }
    });
  };
  sfor<1, TS>(step);

  f32x4 p[2][4];
  f32x2 d[4][4], S, Q, pA, pB;
  float piv, pvb[4], z, z2;
  u32x3 q[4];
  unsigned long long hbad;
  float scl[4], shf[4];
  u32x2 o[8];
  auto stA = [&](int a, int i) { *(LDS_AS float*)(lds + aa + i * API + ((a & 7) * 4 + (a >> 3)) * 256) = acc[a & 7][a >> 3][i]; };
  auto rdP = [&](int e, int n) { p[n >> 2][n & 3] = *(const LDS_AS f32x4*)(lds + ((n & 2) ? la1 : la0) + (n & 1) * PREG + (n >> 2) * 4096 + e * 256); };
  auto rdZ = [&](int e) { piv = *(const LDS_AS float*)(lds + lq + e * 256); };
  auto sumP = [&](int e, int h) {
    d[e][2 * h] = (lo2(p[h][0]) + lo2(p[h][1])) + (lo2(p[h][2]) + lo2(p[h][3]));
    d[e][2 * h + 1] = (hi2(p[h][0]) + hi2(p[h][1])) + (hi2(p[h][2]) + hi2(p[h][3]));
  };
  auto stat = [&](int e, int h) {
    f32x2 &d0 = d[e][2 * h], &d1 = d[e][2 * h + 1];
    d0 = fma2(d0, h ? mB : mA, -(h ? pB : pA));
    d1 = fma2(d1, f32x2{mB[1], mB[1]}, -f32x2{pB[1], pB[1]});
    S = h ? (S + d0) + d1 : d0 + d1;
    Q = fma2(d1, d1, h ? fma2(d0, d0, Q) : d0 * d0);
    if (h) { z = S[0] + S[1]; z2 = Q[0] + Q[1]; }
  };
  auto pub = [&](int e, int t) {
    if (t == 0 || t == 6) { auto r = __builtin_amdgcn_permlane32_swap(fu(z), fu(z2), false, false); z = uf(r[0]); z2 = uf(r[1]); }
    if (t == 1) z += z2;
    if (t == 2) z += dpp<0x128>(z);
    if (t == 3) z += dpp<0x124>(z);
    if (t == 4) { auto r = __builtin_amdgcn_permlane16_swap(fu(z), fu(z2), false, false); z = uf(r[0]); z2 = uf(r[1]); }
    if (t == 5) z = uf(min(fu(z + z2), INVALID - 1));
    if (t == 7) __builtin_amdgcn_raw_buffer_store_b96(u32x3{fu(z), fu(pvb[e]), fu(z2)}, rg, gs + e * GST, 0, 16);
    else PIN(z);
  };
  auto ldG = [&](int e) {
    asm volatile("buffer_load_dwordx3 %0, %1, %2, 0 offen offset:%3 sc1" : "=v"(q[e]) : "v"(go), "s"(rg), "n"(e * GST) : "memory");
  };
  auto bad = [&](int e, int n) {
    asm volatile("s_waitcnt vmcnt(%1)" : "+v"(q[e]) : "n"(n) : "memory");
    return __ballot(max(max(q[e][0], q[e][1]), q[e][2]) == INVALID);
  };
  auto poll = [&](int e, int n) {
    if (bad(e, n)) {
#pragma nounroll
      for (int sp = 0; sp < SPIN_CAP; ++sp) {
        __builtin_amdgcn_s_sleep(SLP);
        ldG(e);
        if (!bad(e, 0)) break;
      }
      if (e == 1) { PIN(q[2]); ldG(2); }
    }
  };
  auto coef = [&](int e) {
    const u32x3 v = q[e];
    const float dl = uf(v[1]) - pvb[e];
    float a1 = __builtin_fmaf(nrow, dl, uf(v[0])), a2 = __builtin_fmaf(dl, uf(v[0]) + a1, uf(v[2]));
    sum16(a1, a2);
    const float dm = a1 * (1.0f / (float)L);
    scl[e] = __builtin_amdgcn_rsqf(__builtin_fmaf(-dm, dm, __builtin_fmaf(a2, 1.0f / (float)L, 1e-5f))) * bfel(ga4, e);
    shf[e] = __builtin_fmaf(-dm, scl[e], bfel(be4, e));
  };
  auto gelu = [&](int e, int i0, int i1) {
#pragma unroll
    for (int i = i0; i < i1; ++i) {
      const f32x2 u = d[e][i] * scl[e] + shf[e];
      const f32x2 t = u * (u * u * -0.1029432397f - 2.3022081985f);
      const f32x2 r = f32x2{__builtin_amdgcn_exp2f(t[0]), __builtin_amdgcn_exp2f(t[1])} + 1.0f;
      d[e][i] = f32x2{__builtin_amdgcn_rcpf(r[0]), __builtin_amdgcn_rcpf(r[1])} * u;
    }
#pragma unroll
    for (int i = 2 * i0; i < 2 * i1; ++i) {
      if (e & 1) o[i][e >> 1] = __builtin_bit_cast(unsigned, bf16x2{(__bf16)d[e - 1][i >> 1][i & 1], (__bf16)d[e][i >> 1][i & 1]});
      if (e == 3) __builtin_amdgcn_raw_buffer_store_b64(o[i], ro, i == 0 ? oz : i == 4 ? oy : oo, (i >> 2) * 4096 + (i & 3) * 8192, OAUX);
      else if (e == 1) PIN(o[i][0]);
      else PIN(d[e][i >> 1]);
    }
  };
  constexpr int NS = 60 - TS - NX;
  static_assert(TC.v[130] >= 12 * NS && TC.v[194] >= 16 * NS, "");
  auto fin = [&](auto AC) {
    constexpr int a = decltype(AC)::value, e = a >> 3, m = a & 7, r = a < 9 ? 0 : (a - 9) >> 3;
    sfor<0, 8>([&](auto JC) {
      constexpr int j = decltype(JC)::value, s = TS + ((a & 1) ? 7 - j : j), f = a < 9 || (j >= 2 && j <= 5) ? -1 : ((a - 9) & 7) * 4 + (j & 3);
      constexpr int fp = pasof(s) & 1, fi = m + posof(s), u0 = TC.v[8 * a + j], u1 = TC.v[8 * a + j + 1];
      SCHED();
      mfma<(s >= FAS)>(acc[m][e], B[s][e], F[fp][fi]);
      SCHED();
      if constexpr (a == 0 && j < 6) F[1][8 + j] = ldF(3, 8 + j);
      if (a == 1 && j == 7) { if (59 >= FAS) asm volatile("" :: "a"(F[1][13])); else PIN(F[1][13]); }
      if (a > 0 && j >= 2 && j <= 5) stA((a + 31) & 31, j - 2);
      if (f == 1 || (r == 2 && f == 16)) lds_barrier();
      if (f == 2) rdZ(r);
      if (f >= 3 && f <= 10) rdP(r, f - 3);
      if (r == 2 && f == 18) rdZ(3);
      if (r == 2 && (f == 19 || f == 20 || f == 21 || f == 24)) rdP(3, f == 24 ? 3 : f - 19);
      if (a == HA && j == 1) hr = __hip_atomic_load(min_, RLX_AGENT);
      if (a == 24 && j == 7) ldG(0);
      if (f == 26) pub(r, 7);
      SCHED();
      if (a == 24 && j == 6) { hbad = __ballot((unsigned)hr != (unsigned)(ho >> 32)); asm volatile("" :: "s"(hbad)); }
      if (a == 25 && j == 0) { hbad |= __ballot((unsigned)(hr >> 32) != hch); asm volatile("" :: "s"(hbad)); }
      if (f == 14) PIN(p[1][3]);
      if (f == 7 || (r == 2 && f == 24)) PIN(piv);
      if (f == 8 || (r == 2 && f == 26)) { piv += dpp<0x128>(piv); PIN(piv); }
      if (f == 9) { pvb[r] = bfel(bi4, r); PIN(pvb[r]); }
      if (f == 10) { piv += dpp<0x124>(piv); PIN(piv); }
      if (f == 11) { pvb[r] = uf(min(fu(pvb[r] + piv), INVALID - 1)); PIN(pvb[r]); }
      if (r == 2 && f == 20) { pvb[3] = bfel(bi4, 3); PIN(pvb[3]); }
      if (f == 15) {
        pA = mA * piv; pB = mB * piv;
        sumP(r, 0); sumP(r, 1); stat(r, 0); stat(r, 1);
        PIN(z); PIN(z2);
      }
      if (f == 17 || f == 18) pub(r, f - 17);
      if (f >= 21 && f <= 23) pub(r, f - 18);
      if (f == 25) pub(r, 6);
      SCHED();
#pragma unroll
      for (int u = u0; u < u1; ++u) {
        if (u < 12 * NS) shuf(TS + NX + u / 12, 1, u % 12);
        else if (u < 16 * NS) shuf(TS + NX + (u - 12 * NS) / 4, 1, 12 + (u & 3));
      }
      SCHED();
      if (f == 19) pub(r, 2);
    });
  };
  sfor<0, 32>(fin);
  STAMP(2);
  SCHED();
  ldG(1);
  SCHED();
  sumP(3, 0);
  piv += dpp<0x124>(piv);
  PIN(piv); PIN(d[3][0]); PIN(d[3][1]);
  SCHED();
#pragma unroll
  for (int i = 0; i < 4; ++i) { stA(31, i); SCHED(); }
  pA = mA * piv; pB = mB * piv;
  stat(3, 0);
  pvb[3] = uf(min(fu(pvb[3] + piv), INVALID - 1));
  hbad |= bad(0, 2);
  asm volatile("" :: "s"(hbad), "v"(pvb[3]));
  PIN(S); PIN(Q);
  SCHED();
  lds_barrier();
#pragma unroll
  for (int i = 4; i < 8; ++i) rdP(3, i);
  SCHED();
  coef(0);
  PIN(scl[0]); PIN(shf[0]);
  SCHED();
  sumP(3, 1); stat(3, 1);
  STAMP(3);
#pragma unroll
  for (int t = 0; t < 8; ++t) pub(3, t);
  SCHED();
  STAMP(7);
  if (__builtin_expect(hbad != 0, 0)) {
    unsigned sent = (unsigned)(ho >> 32);
    asm volatile("s_waitcnt vmcnt(0)" :: "v"(q[1]) : "memory");
#pragma nounroll
    for (int sp = 0; sp < SPIN_CAP; ++sp) {
      if (__any((unsigned)hr != sent)) { __hip_atomic_store(mout, (hr << 32) | hch, RLX_AGENT); sent = (unsigned)hr; }
      if (__all((unsigned)(hr >> 32) == hch)) break;
      __builtin_amdgcn_s_sleep(1);
      hr = __hip_atomic_load(min_, RLX_AGENT);
    }
    asm volatile("s_waitcnt vmcnt(0)" :: "v"(hr) : "memory");
    ldG(0); ldG(1);
    poll(0, 0);
    coef(0);
    asm volatile(".Lconv_code_end:" :: "v"(scl[0]), "v"(shf[0]));
  }
  gelu(0, 0, 4);
  SCHED();
  ldG(2);
  XST(0);
  poll(1, 2);
  XST(1);
  coef(1);
  gelu(1, 0, 4);
  SCHED();
  XST(2);
  poll(2, 0);
  ldG(3);
  XST(3);
  SCHED();
  coef(2);
  gelu(2, 0, 4);
  XST(4);
  poll(3, 0);
  XST(5);
  coef(3);
  STAMP(4);
  gelu(3, 0, 2);
  SCHED();
  gelu(3, 2, 4);
  STAMP(5);
}

extern "C" void kernel_launch(void* const* d_in, const int* in_sizes, int n_in,
                              void* d_out, int out_size, void* d_ws, size_t ws_size,
                              hipStream_t stream) {
  const __bf16* x = (const __bf16*)d_in[0];
  const __bf16* w = (const __bf16*)d_in[1];
  const __bf16* bias = (const __bf16*)d_in[2];
  const __bf16* gamma = (const __bf16*)d_in[3];
  const __bf16* beta = (const __bf16*)d_in[4];
  unsigned long long* gran = (unsigned long long*)d_ws;
  unsigned long long* stamps = (unsigned long long*)((char*)d_ws + (size_t)32 * 1024 * 1024);
  const unsigned F = 0x00020000u, XN = (2 * L + KT - 2) * D * 2;
  conv_gn_gelu_kernel<<<256, 256, 0, stream>>>(w, KT * D * D * 2, F, x, XN, F, x + T * D, XN, F, d_out, L * D * 2, F, (__bf16*)d_out + L * D, L * D * 2, F,
                                               gran, GRAN_BYTES, F, bias, gamma, beta, stamps);
}
```

```cpp
#include <hip/hip_runtime.h>
#include <type_traits>
#include <utility>

typedef __bf16 bf16x2 __attribute__((ext_vector_type(2)));
typedef __bf16 bf16x4 __attribute__((ext_vector_type(4)));
typedef __bf16 bf16x8 __attribute__((ext_vector_type(8)));
typedef float f32x4 __attribute__((ext_vector_type(4)));
typedef float f32x2 __attribute__((ext_vector_type(2)));
typedef unsigned u32x2 __attribute__((ext_vector_type(2)));
typedef unsigned u32x3 __attribute__((ext_vector_type(3)));
typedef unsigned u32x4 __attribute__((ext_vector_type(4)));
#define LDS_AS __attribute__((address_space(3)))
#define GAS __attribute__((address_space(1)))
#define DI __device__ __forceinline__

constexpr int T = 4000, D = 512, KT = 15, L = 1993;
constexpr int XPL = 544, XRES = 4 * XPL, XIMG = 8 * XRES, WREG = 2 * XIMG;
constexpr int LDS_BYTES = 4 * WREG;
constexpr int API = 8256, PREG = WREG + 16;
constexpr int SPIN_CAP = 1 << 20;
constexpr unsigned INVALID = 0xFFFFFFFFu;
constexpr int GST = 128;
constexpr int GRAN_BYTES = 4096 * GST;

#define RLX_AGENT __ATOMIC_RELAXED, __HIP_MEMORY_SCOPE_AGENT
#define SCHED() __builtin_amdgcn_sched_barrier(0)
#define FAS 46
#define PF1 6
#define PF2 20
#define PF3 36
#define GB 43
#define RB 4
#define SLP 1
#define HG 46
#define HA 12
#define OAUX 18
#define XPF 0
#define MAP 2
#define PFM 0
#define PD 5
#define PG 2
#define NTX 0
#define PF0 0
#define PFN 16
#define TOP 1
#define SHT 0
#define PFS 1
#define BX 0
#define WP 0
#define WQ 1
#define CP0 2
#define PFH 0
#define PEN 9
#define PFA 1
#define PFB 13
#define PFC 28
#define PFD 41
#define SOF(x) (x)
#define CT 1
#define TR 1
#define EP 1
#define NX 0
constexpr int TS = 52;
constexpr int SU = 42;
#define RING 3
#define NPRE 8
#define NQ 3
#define HS 11
#define HD 4
#define XD 3
#define CO 4
#define QW 1
constexpr unsigned woff(int S) {
  return S < 16 ? (unsigned)((2 * (S & 7)) * (D * D * 2) + (S >> 3) * 32768)
                : (unsigned)((2 * ((S - 16) % 7) + 1) * (D * D * 2) + ((S - 16) / 7) * 32768);
}
constexpr int pasof(int G) { return G % 30 < 8 ? 0 : G % 30 < 16 ? 1 : G % 30 < 23 ? 2 : 3; }
constexpr int posof(int G) { return G % 30 - (pasof(G) == 0 ? 0 : pasof(G) == 1 ? 8 : pasof(G) == 2 ? 16 : 23); }
constexpr int NPF = MAP >= 3 ? 7 : 3, PFP[7] = {PF1, PF2, PF3, PFA, PFB, PFC, PFD};
constexpr int pfu(int G) {
  for (int i = 0; i < NPF; ++i)
    if (PFP[i] == G) return i < 3 ? 2 * i + 2 : 2 * i - 5;
  return -1;
}
constexpr unsigned long long qmask() {
  unsigned long long m = 0;
  for (int i = 0; i < NPF; ++i) m |= QW && PFP[i] >= 3 ? 2ull << PFP[i] : 0ull;
  return m;
}
constexpr unsigned long long QM = qmask();
constexpr bool quiet(int G) { return (QM >> G) & 1; }
constexpr int xst(int im, int p) {
  const int a = im == 0 ? XD : im == 1 ? 16 : 30, z = im == 0 ? 15 : im == 1 ? 29 : 45;
  int n = 0;
  for (int g = a; g <= z; ++g) n += !quiet(g - XD);
  int i = (p * (n - 1) + 8) / 16;
  for (int g = a; g <= z; ++g)
    if (!quiet(g - XD) && i-- == 0) return g;
  return -1;
}
constexpr int xq(int G, int n) {
  for (int im = 0; im < 3; ++im)
    for (int p = 0; p < 17; ++p)
      if (xst(im, p) == G && n-- == 0) return im * 32 + p;
  return -1;
}
constexpr int wcnt(int G) {
  int n = 4 * (G + RING + 1) + (G > GB ? RB * (G - GB) : 0);
  for (int i = 0; i < NPF; ++i) {
    const int d = PFP[i] < 3 ? 9 : G - 1 - PFP[i];
    n += d <= 0 ? (4 * QW + d > 0 ? 4 * QW + d : 0) : (4 * QW - 4 * d > 0 ? 4 * QW - 4 * d : 0);
  }
  return n < 240 ? n : 240;
}
constexpr int qdone(int q) {
  int n = 0;
  for (int i = 0; i < q; ++i) {
    if (2 * i == WP) n += WQ < i ? WQ : i;
    n += 2 * i < WP ? 0 : i == 8 ? 8 - n : i - n < 2 ? i - n : 2;
  }
  return n;
}
constexpr unsigned wmask(int G) {
  int wn = wcnt(G + 1) - wcnt(G);
  const int pos[8] = {14, 18, 22, 26, xq(G + XD, 0) < 0 ? 12 : -1, xq(G + XD, 1) < 0 ? 28 : -1, 30, 24};
  unsigned m = 0;
  for (int i = 0; i < 8; ++i)
    if (pos[i] >= 0 && wn > 0) { m |= 1u << pos[i]; --wn; }
  return m;
}
constexpr int fq(int G, int k) {
  const int PS = pasof(G), J = posof(G), NT = PS < 2 ? 8 : 7, JN = J - NT + 4 + (PS == 2);
  if (G == 1) return k == 0 ? 9 : k == 2 ? 10 : -1;
  if (J == 0 && k == 0) return 8;
  if (k == (J == 0 ? 2 : 0) && J + 2 < NT) return 9 + J;
  if ((k == 2 || k == 4) && JN >= 0 && JN < 4) return 16 + 2 * JN + (k == 4);
  return -1;
}
struct Tab { int v[512]; };
constexpr Tab slist() {
  Tab t{};
  int n = 0;
  for (int h = 0; h < 2 * (TS - SU) + NX + 58 - TS; ++h) {
    const int x = h - 2 * (TS - SU), G = SU + (h >> 1);
    const int s = x < 0 ? ((G + 1) & ~1) + (h & 1) : x < NX ? TS + x : TS + 2 + x - NX, j = x < 0 ? (G + 1) & 1 : x < NX;
    if (!j) t.v[++n] = s * 64 + 16;
    for (int i = 0; i < 16; ++i) t.v[++n] = s * 64 + j * 32 + i;
  }
  t.v[0] = n;
  return t;
}
constexpr Tab SL = slist();
constexpr Tab scum() {
  Tab t{};
  int n = SL.v[0];
  t.v[(TS - SU) * 32] = n;
  for (int G = TS - 1; G >= SU; --G) {
    const unsigned wm = wmask(G) | (xq(G, 0) >= 0 ? 0x154u : 0u) | (xq(G, 1) >= 0 ? 0x1110000u : 0u) | (wmask(G) ? 0x400u : 0u) |
                        (G == TS - 2 ? 0x14111000u : 0u) | (G == HG ? 1u << 20 : 0u) | (xq(G + XD, 0) >= 0 ? 1u << 12 : 0u) | (xq(G + XD, 1) >= 0 ? 1u << 28 : 0u);
    for (int k = 31; k >= 0; --k) {
      n -= (k & 1) ? 2 : ((wm >> k) & 1) ? 0 : 2 - (fq(G, k) >= 0) - (k == (posof(G) == 0 ? 8 : 30));
      const int o = (G - SU) * 33 - (G & 1) + (k & ~1) + ((G & 1) ? (k > 0) + (k > 16) : 0);
      if (n < o) n = o;
      t.v[(G - SU) * 32 + k] = n;
    }
  }
  return t;
}
constexpr int flast(int G) { return fq(G, 4) >= 0 ? fq(G, 4) : fq(G, 2) >= 0 ? fq(G, 2) : fq(G, 0); }
constexpr int tocc(int a, int j) {
  if (a == 0) return j < 6;
  if (j >= 2 && j <= 5) return 2;
  if (a < 9) return a == 1 && j == 7;
  if (a == HA && j == 1) return 2;
  const int occ[32] = {0, 2, 1, 1, 1, 1, 1, 2, 2, 2, 2, 2, 0, 0, 1, 2, 0, 2, 1, 1, 0, 1, 2, 2, 0, 2, 1};
  return occ[((a - 9) & 7) * 4 + (j & 3)];
}
constexpr Tab tcum() {
  Tab t{};
  for (int u = 0; u < 256; ++u) t.v[u + 1] = t.v[u] + 2 - tocc(u >> 3, u & 7);
  return t;
}
constexpr Tab SC = scum(), TC = tcum();
static_assert(SC.v[0] == 0 && !(SU & 1), "");
template <int A, int... I, class FT> DI void sfor_(std::integer_sequence<int, I...>, FT&& f) { (f(std::integral_constant<int, A + I>{}), ...); }
template <int A, int Z, class FT> DI void sfor(FT&& f) { sfor_<A>(std::make_integer_sequence<int, Z - A>{}, f); }

DI unsigned xsel(unsigned oth, unsigned own) {
  unsigned r;
  register unsigned long long m asm("vcc") = 0xFF00FF00FF00FF00ull;
  asm volatile("v_cndmask_b32_dpp %0, %1, %2, vcc row_ror:8 row_mask:0xf bank_mask:0xf" : "=v"(r) : "v"(oth), "v"(own), "s"(m));
  return r;
}
template <bool FA>
DI void mfma(f32x4& acc, const u32x4& a, const bf16x8& b) {
  if (FA) asm volatile("v_mfma_f32_16x16x32_bf16 %0, %1, %2, %0" : "+a"(acc) : "v"(a), "a"(b));
  else asm volatile("v_mfma_f32_16x16x32_bf16 %0, %1, %2, %0" : "+a"(acc) : "v"(a), "v"(b));
}
DI void mfma0(f32x4& acc, const u32x4& a, const bf16x8& b) {
  asm volatile("v_mfma_f32_16x16x32_bf16 %0, %1, %2, 0" : "=a"(acc) : "v"(a), "v"(b));
}
DI void setm0(unsigned v) { asm volatile("s_mov_b32 m0, %0" :: "s"(v) : "memory"); }
DI void st_tid(unsigned v, int off) { asm volatile("ds_write_addtid_b32 %0 offset:%1" :: "v"(v), "n"(off) : "memory"); }
DI void lds_barrier() {
  asm volatile("" ::: "memory");
  __builtin_amdgcn_s_waitcnt(0xC07F);
  __builtin_amdgcn_s_barrier();
  asm volatile("" ::: "memory");
}
DI unsigned fu(float v) { return __builtin_bit_cast(unsigned, v); }
DI float uf(unsigned v) { return __builtin_bit_cast(float, v); }
DI f32x2 lo2(const f32x4& v) { return f32x2{v[0], v[1]}; }
DI f32x2 hi2(const f32x4& v) { return f32x2{v[2], v[3]}; }
DI f32x2 fma2(f32x2 a, f32x2 b, f32x2 c) { return __builtin_elementwise_fma(a, b, c); }
DI float bfel(const u32x2& v, int e) { return uf((e & 1) ? v[e >> 1] & 0xFFFF0000u : v[e >> 1] << 16); }
#define PIN(x) asm volatile("" :: "v"(x))
template <int CTRL>
DI float dpp(float v) { return __builtin_bit_cast(float, __builtin_amdgcn_update_dpp(0, __builtin_bit_cast(int, v), CTRL, 0xF, 0xF, true)); }

DI void sum16(float& a, float& b) {
  auto p = __builtin_amdgcn_permlane32_swap(fu(a), fu(b), false, false);
  float v = uf(p[0]) + uf(p[1]);
  v += dpp<0x128>(v);
  v += dpp<0x124>(v);
  auto r = __builtin_amdgcn_permlane16_swap(fu(v), fu(v), false, false);
  v = uf(r[0]) + uf(r[1]);
  auto q = __builtin_amdgcn_permlane32_swap(fu(v), fu(v), false, false);
  a = uf(q[0]); b = uf(q[1]);
}

#define SST(k) do {} while (0)
#define XST(k) do {} while (0)
#define STAMP(k) do {} while (0)

__global__ __launch_bounds__(256, 1)
void conv_gn_gelu_kernel(const void* w, unsigned wn, unsigned wf, const void* x0, unsigned x0n, unsigned x0f, const void* x1, unsigned x1n, unsigned x1f,
                         void* o0, unsigned o0n, unsigned o0f, void* o1, unsigned o1n, unsigned o1f, unsigned long long* gran, unsigned gn, unsigned gf,
                         const void* bi, const void* ga, const void* be,
                         unsigned long long* __restrict__ stamps) {
  typedef const __amdgpu_buffer_rsrc_t __attribute__((address_space(4)))* KA;
  const KA ka = (KA)__builtin_amdgcn_kernarg_segment_ptr();
  const __amdgpu_buffer_rsrc_t rw = ka[0], rx = ka[1 + ((blockIdx.x >> (MAP == 3 ? 2 : MAP ? 1 : 0)) & 1)];
  SCHED();
  __shared__ __attribute__((aligned(1024))) char smem[LDS_BYTES];
  LDS_AS char* const lds = (LDS_AS char*)smem;
  const int tid = threadIdx.x, lane = tid & 63;
  const int wv = __builtin_amdgcn_readfirstlane(tid >> 6);
  const int bid = blockIdx.x;
  const int xcd = bid & 7, idx = bid >> 3;
  const int mt = MAP == 4 ? (xcd & 3) * 8 + (idx & 7) : MAP == 3 ? (xcd >> 1) * 8 + (idx & 7) : ((xcd >> (MAP ? 1 : 0)) & 1) * 16 + (idx & 15);
  const int nt = MAP == 4 ? (xcd & 4) + (idx >> 3) : MAP == 3 ? (xcd & 1) * 4 + (idx >> 3) : (MAP ? (xcd & 1) * 4 + (xcd >> 2) * 2 : xcd & 6) + (idx >> 4);
  const int b = mt >> 4, sh = (SHT >> 4 * nt) & 7, l0 = (mt & 15) * 128 + sh, n0 = (nt ^ NTX) * 64;
  const int c = lane & 15, g = lane >> 4;
  STAMP(0);

  auto ldC = [&](int skip) {
    unsigned long long pc;
    unsigned len;
    asm volatile("s_getpc_b64 %0\n\ts_mov_b32 %1, .Lconv_code_end-." : "=s"(pc), "=s"(len));
    const __amdgpu_buffer_rsrc_t rc = __builtin_amdgcn_make_buffer_rsrc((void*)pc, 0, len, 0x00020000);
    return __builtin_amdgcn_raw_buffer_load_b32(rc, (unsigned)((lane * 128 + idx * 4 + wv) * 128) | (skip == 1 && lane ? 0x80000000u : 0u), skip == 2 ? 12288 : 0, 0);
  };
  unsigned cpf, cp0 = 0;
  if (CP0 == 1) cpf = ldC(0);
  if (CP0 == 2) cp0 = ldC(1);
  SCHED();
  unsigned wo = (unsigned)((2 * g + 16 * (c >> 3)) * 1024 + n0 * 2 + (c & 7) * 16 + wv * 65536), wo2 = wo + 8192;
  unsigned pfe = PFH == 2 ? (unsigned)(((lane >> 2) * 2 * D + (lane & 2) * 16 + idx) * (D * 2) + (n0 & ~64) * 2 + (lane & 1) * 128 + (wv + CO) * 65536)
                          : (unsigned)(((lane >> 1) * 2 * D + idx) * (D * 2) + (n0 & ~64) * 2 + (lane & 1) * 128 + (wv + CO) * 65536);
  const int pes = WP || PFH == 2 ? 0 : 2 * D * D * 2;
  unsigned xo0 = (unsigned)((2 * l0 + 16 * (lane >> 3)) * (D * 2) + ((lane ^ (lane >> 1)) & 4) * 16 + (lane & 3) * 16 + (wv + CO) * 128);
  const bool last = (mt & 15) == 15;
  unsigned xoh = xo0 + ((last && (lane >> 4) == 2) ? (unsigned)(-(2 * l0 + 64) * (D * 2)) : 131072u);
  if (TOP) { asm volatile("" : "+v"(wo), "+v"(wo2), "+v"(pfe), "+v"(xo0), "+v"(xoh)); SCHED(); }

  const int grp = b * 8 + nt;
  const int gbase = (grp * 4 + wv) * 64 * GST;
  __amdgpu_buffer_rsrc_t rg, ro;
  GAS unsigned long long *min_, *mout;
  const char *bias, *gamma, *beta;

  u32x4 raw[60][4];
  unsigned so[60];
  auto ldW = [&](int n) {
    raw[n >> 2][n & 3] = __builtin_bit_cast(u32x4, __builtin_amdgcn_raw_buffer_load_b128(rw, ((n & 2) ? wo2 : wo) + (n & 1) * 1024, so[n >> 2], 0));
  };
#pragma unroll
  for (int i = 0; i < RING + 2; ++i) so[i] = woff(i) + CO * 65536;
  unsigned pe = 0;
  if (PF0 == 1) pe = __builtin_amdgcn_raw_buffer_load_b32(rw, pfe, pes, 0);
  SCHED();
#pragma unroll
  for (int i = 0; i < 4; ++i) if (!WP) ldW(i);
  SCHED();
  if (PF0 == 2) pe = __builtin_amdgcn_raw_buffer_load_b32(rw, pfe, pes, 0);
  SCHED();
  SST(0);

  unsigned xo1 = (unsigned)((2 * l0 + 256 + 2 * (lane >> 3)) * (D * 2) + (lane & 7) * 16 + (wv + CO) * 128);
  const unsigned xa1 = (unsigned)(wv * WREG + (lane >> 3) * XRES + 512 + (lane & 7) * 4);
  auto ldX = [&](int pc) -> u32x4 {
    return __builtin_bit_cast(u32x4, __builtin_amdgcn_raw_buffer_load_b128(rx, pc == 16 ? xo1 : (pc & 1) ? xoh : xo0, (pc & 14) * 1024, 0));
  };
  auto stX = [&](int p, int pc, const u32x4& v, int d) {
    if (pc == 16) *(LDS_AS unsigned*)(lds + xa1 + p * XIMG + d * XPL) = v[d];
    else st_tid(v[d], p * XIMG + (pc >> 1) * XRES + d * XPL + (pc & 1) * 256);
  };
  u32x4 xs[32];
  unsigned long long hr;
  auto inval = [&]() { __builtin_amdgcn_raw_buffer_store_b64(u32x2{INVALID, INVALID}, rg, lane * 8, gbase + (mt & 15) * 4 * GST, 16); };
  auto ldS = [&](int n) {
    if (n < 16) {
      xs[n & 15] = ldX(n);
      if (n == WP - 1) { SCHED(); ldW(0); ldW(1); ldW(2); ldW(3); }
    }
    else if (n < 20) ldW(n - 12);
    else if (n == 20) xs[16] = ldX(16);
    else if (n < 21 + 4 * (RING - 1)) {
      ldW(n - 13);
      if (n == 20 + 4 * (HS - 1)) { inval(); hr = __hip_atomic_load(min_, RLX_AGENT); }
    }
    if (PF0 == 4 && n == PEN) { SCHED(); pe = __builtin_amdgcn_raw_buffer_load_b32(rw, pfe, pes, 0); }
  };
#pragma unroll
  for (int i = 0; i < NPRE; ++i) { ldS(i); SCHED(); }
  if (PF0 == 3) pe = __builtin_amdgcn_raw_buffer_load_b32(rw, pfe, pes, 0);
  SCHED();
  typedef char* const __attribute__((address_space(4)))* KP;
  int b2 = b;
  asm volatile("" : "+s"(b2));
  rg = ka[5]; ro = ka[3 + b2];
  bias = ((KP)ka)[12]; gamma = ((KP)ka)[13]; beta = ((KP)ka)[14];
  GAS unsigned long long* mbase = (GAS unsigned long long*)((KP)ka)[10];
  SCHED();
  setm0(wv * WREG);

  unsigned pfr[8];
  unsigned pfw = MAP >= 3 ? (unsigned)(((lane >> 2) * 2 * D + idx) * (D * 2) + (n0 & 256) * 2 + (lane & 3) * 128 + wv * 65536) : PFS ? (unsigned)(((lane >> 2) * 2 * D + idx * 2 + ((lane >> 1) & 1)) * (D * 2) + (n0 & ~64) * 2 + (lane & 1) * 128 + wv * 65536)
                     : (unsigned)(((lane >> 2) * 2 * D + (mt & 15) * 4 + (lane & 3)) * (D * 2) + n0 * 2 + wv * 65536);

  const unsigned pfx = lane < 16 ? (unsigned)((2 * l0 + 32 * nt + 2 * lane) * (D * 2) + wv * 128) : 0x80000000u;
  unsigned px = 0;
  const int ei = lane & 3, cq = c >> 2, ch0 = CT ? n0 + 16 * wv + 4 * ei : n0 + 32 * (wv & 1) + 4 * (wv >> 1) + 8 * ei;
  const int lb = CT ? (2 * (wv & 1) + (ei >> 1)) * API + ((wv >> 1) + 2 * (ei & 1)) * 64 : ei * API + wv * 64;
  unsigned aa = (unsigned)(wv * PREG + lane * 4);
  unsigned la0 = (unsigned)(lb + g * 1024 + cq * 16), la1 = la0 + 2 * PREG;
  unsigned lq = (unsigned)(lb + cq * PREG + (CT ? ei & 1 : ei >> 1) * 4);
  unsigned gs = lane >= 4 ? 0x80000000u : (unsigned)(gbase + (mt & 15) * 4 * GST + ei * 16);
  unsigned go = (unsigned)(gbase + (lane >> 2) * 4 * GST + ei * 16);
  unsigned oo = (unsigned)((l0 + 32 * cq + g) * (D * 2) + ch0 * 2);
  const int lim = (!last || cq < 2 ? 99 : cq == 2 ? 9 - sh : sh) - g;
  const unsigned ow = last && cq == 3 ? (unsigned)((l0 + 96) * (D * 2)) : 0u;
  unsigned oz = oo - (lim > 0 ? ow : 0u), oy = oo - (lim > 4 ? ow : 0u);
  if (TR) {
    oz = (unsigned)(cq * (PREG + 16) + g * 128 + wv * 32 + ei * 8);
    oy = (unsigned)(wv * (PREG + 16) + lane * 16);
    oo = (unsigned)((l0 + 32 * wv + (lane >> 3)) * (D * 2) + n0 * 2 + (lane & 7) * 16);
  }
  f32x2 mA = {lim > 0 ? 1.f : 0.f, lim > 8 ? 1.f : 0.f}, mB = {lim > 4 ? 1.f : 0.f, lim > 12 ? 1.f : 0.f};
  unsigned po = (unsigned)(ch0 * 2);
  float nrow = lane >= 60 ? (float)(L - 15 * 128) : 128.f;
  asm volatile("" : "+v"(po), "+v"(nrow), "+v"(aa), "+v"(la0), "+v"(la1), "+v"(lq), "+v"(gs), "+v"(go), "+v"(oo), "+v"(oz), "+v"(oy), "+v"(mA), "+v"(mB));

  SCHED();
  mbase += GRAN_BYTES / 8 + (grp * 4 + wv) * 256;
  min_ = mbase + (mt & 15) * 16 + c;
  mout = mbase + c * 16 + (mt & 15);
  asm volatile("" : "+s"(ro), "+s"(bias), "+s"(gamma), "+s"(beta));

  unsigned fa[2][2];
#pragma unroll
  for (int hi = 0; hi < 2; ++hi) {
    const int v = c + hi;
    fa[0][hi] = (unsigned)(wv * WREG + g * XPL + v * 32 + (v & 1) * 16);
    fa[1][hi] = fa[0][hi] ^ 16u;
  }
  auto ldF = [&](int pass, int q) -> bf16x8 {
    return *(LDS_AS bf16x8*)(lds + fa[pass & 1][q >> 3] + (pass >> 1) * XIMG + (q & 7) * XRES);
  };
  unsigned U[60][4][2], V[60][4][2];
  u32x4 B[60][4];
  auto mkU = [&](int s, int r, int j) { U[s][r][j] = xsel(raw[s][r][j], raw[s][r][2 + j]); };
  auto mkV = [&](int s, int r, int j) { V[s][r][j] = __builtin_amdgcn_update_dpp(raw[s][r][j], raw[s][r][2 + j], 0x128, 0xF, 0xC, false); };
  auto mkB = [&](int s, int e, int d) {
    const unsigned sel = (e & 1) ? 0x07060302u : 0x05040100u;
    B[s][e][d] = d < 2 ? __builtin_amdgcn_perm(V[s][2 * d + 1][e >> 1], V[s][2 * d][e >> 1], sel)
                       : __builtin_amdgcn_perm(U[s][2 * d - 3][e >> 1], U[s][2 * d - 4][e >> 1], sel);
  };

  f32x4 acc[8][4];
  bf16x8 F[2][15];
  sfor<0, 9>([&](auto QC) {
    constexpr int q = decltype(QC)::value, q0 = qdone(q), q1 = qdone(q + 1), qa = 2 * q == WP ? (WQ < q ? WQ : q) : 0;
    if (q == 0) SST(1);
    if (2 * q == WP) {
      SCHED();
#pragma unroll
      for (int i = 0; i < 8; ++i) mkU(0, i >> 1, i & 1);
      SCHED();
      SST(2);
#pragma unroll
      for (int i = 0; i < 8; ++i) mkV(0, i >> 1, i & 1);
#pragma unroll
      for (int i = 0; i < 16; ++i) mkB(0, i >> 2, i & 3);
      SCHED();
#pragma unroll
      for (int i = 0; i < 4 * qa; ++i) mfma0(acc[i >> 2][i & 3], B[0][i & 3], F[0][i >> 2]);
      SCHED();
    }
    if (q < 8) {
#pragma unroll
      for (int i = 0; i < 8; ++i) stX(0, 2 * q + (i >> 2), xs[2 * q + (i >> 2)], i & 3);
      F[0][q] = ldF(0, q);
      if (q == 0) SST(3);
      if (q == 3) SST(4);
      if (q == 7) SST(5);
    }
#pragma unroll
    for (int e = 0; e < (q1 - q0 - qa > 1 ? 4 * (q1 - q0 - qa) : 4); ++e) {
      SCHED();
      if (q0 + qa + (e >> 2) < q1) mfma0(acc[q0 + qa + (e >> 2)][e & 3], B[0][e & 3], F[0][q0 + qa + (e >> 2)]);
      SCHED();
      if (e < NQ) ldS(NPRE + NQ * q + e);
    }
  });
  static_assert(NPRE + NQ * 9 >= 21 + 4 * (RING - 1), "");
  xo0 += 1024; xoh += 1024; xo1 += 1024;
  SCHED();
#pragma unroll
  for (int i = 0; i < 8; ++i) mkU(1, i >> 1, i & 1);
  SCHED();
  SST(6);
#pragma unroll
  for (int i = 0; i < 8; ++i) mkV(1, i >> 1, i & 1);
#pragma unroll
  for (int i = 0; i < 4; ++i) stX(0, 16, xs[16], i);
  F[0][8] = ldF(0, 8);
#pragma unroll
  for (int i = 0; i < 16; ++i) mkB(1, i >> 2, i & 3);
  if (PF0 && PEN < 20) PIN(pe);
  if (CP0 == 2) PIN(cp0);
  SCHED();
#pragma unroll
  for (int i = 0; i < 4; ++i) ldW(wcnt(0) + i);
  xs[0] = ldX(0);
  if (xq(XD, 1) >= 0) xs[1] = ldX(1);
  unsigned pf = 0;
  if (PFH == 0) pf = __builtin_amdgcn_raw_buffer_load_b32(rw, pfw, CO * 65536 + (MAP >= 3 ? woff(RING + 2) : 0), 0);
  if (PFH == 1) pf = __builtin_amdgcn_raw_buffer_load_b32(rw, pfe, 32768, 0);
  if (XPF & 1) px = __builtin_amdgcn_raw_buffer_load_b32(rx, pfx, CO * 128 + 1024, 0);
  if (CP0 != 1) cpf = ldC(CP0 ? 2 : 0);
  u32x2 bi4, ga4, be4;
  SCHED();
  STAMP(1);
  unsigned hch;
  unsigned long long ho;

  auto shuf = [&](int s, int j, int t) {
    if (t >= 8) mkB(s, 2 * j + ((t >> 2) & 1), t & 3);
    else if (t & 1) mkV(s, t >> 1, j);
    else mkU(s, t >> 1, j);
  };
  auto step = [&](auto GC) {
    constexpr int G = decltype(GC)::value;
    constexpr int PS = pasof(G), J = posof(G);
    constexpr int W0 = xq(G, 0), W1 = xq(G, 1), L0 = xq(G + XD, 0), L1 = xq(G + XD, 1);
    constexpr int WA = wcnt(G), WN = wcnt(G + 1) - WA;
    constexpr unsigned WM = wmask(G);
    constexpr int PU = pfu(G), PV = pfu(G - 5);
    constexpr int G0 = G & ~1, N0 = (G + 1) & ~1, NJ = (G + 1) & 1;
    static_assert(xq(G, 2) < 0 && WN >= 0 && WN == __builtin_popcount(WM), "");
    static_assert(W0 < 0 || G < ((W0 >> 5) == 0 ? 12 : (W0 >> 5) == 1 ? 26 : 42) + ((W0 & 31) >> 2), "");
    static_assert(W1 < 0 || G < ((W1 >> 5) == 0 ? 12 : (W1 >> 5) == 1 ? 26 : 42) + ((W1 & 31) >> 2), "");
    static_assert(wcnt(TS - 1) == 240, "");

    sfor<0, 32>([&](auto KC) {
      constexpr int k = decltype(KC)::value, FQ = fq(G, k), FL = flast(G);
      constexpr int e = G == 1 ? k >> 3 : 2 * (G & 1) + (k >> 4), kk = (k & 16) ? 31 - k : k;
      constexpr int m = G == 1 ? k & 7 : kk >> 1, sj = G == 1 ? 1 : G0 + (kk & 1), fp = pasof(sj) & 1, fi = m + posof(sj);
      SCHED();
      mfma<(sj >= FAS)>(acc[m][e], B[sj][e], F[fp][fi]);
      SCHED();
      if ((k & 1) && G < SU) { shuf(N0 + (k >> 4), NJ, (k - 1) & 15); shuf(N0 + (k >> 4), NJ, k & 15); }
      if constexpr (FQ >= 0) F[(PS + (FQ >> 4)) & 1][FQ & 15] = ldF((PS + (FQ >> 4)) & 3, FQ & 15);
      if ((k == 0 || k == 16) && NJ == 0 && G < SU) PIN(raw[N0 + (k >> 4)][3]);
      if (k >= 2 && k <= 8 && !(k & 1) && W0 >= 0) stX((W0 >> 5) != 1, W0 & 31, xs[W0 & 31], (k - 2) >> 1);
      if ((k == 10 || k == 16 || k == 20 || k == 24) && W1 >= 0) stX((W1 >> 5) != 1, W1 & 31, xs[W1 & 31], k == 10 ? 0 : (k - 12) >> 2);
      if (k == 10) {
#pragma unroll
        for (int s = (WA + 3) >> 2; 4 * s < WA + WN; ++s) asm volatile("s_mov_b32 %0, %1" : "=s"(so[s]) : "n"(SOF(woff(s % 30) + (CO ^ (s / 30 * 4)) * 65536)));
        if (L0 == 32) { xo0 ^= 1536; xoh ^= 1536; xo1 ^= 1536; }
        if (L0 == 64) { xo0 += 1024; xoh += 1024; xo1 += 1024; }
      }
      if (k == 12 && L0 >= 0) xs[L0 & 31] = ldX(L0 & 31);
      if (k == 28 && L1 >= 0) xs[L1 & 31] = ldX(L1 & 31);
      if ((WM >> k) & 1) ldW(WA + __builtin_popcount(WM & ((1u << k) - 1)));
      if constexpr (k == (J == 0 ? 8 : 30) && FL >= 0) {
        if (G - J + (FL >> 4) * (PS < 2 ? 8 : 7) >= FAS) asm volatile("" :: "a"(F[(PS + (FL >> 4)) & 1][FL & 15]));
        else PIN(F[(PS + (FL >> 4)) & 1][FL & 15]);
      }
      if constexpr (PU >= 0) {
        if (k == 30) pfr[PU] = __builtin_amdgcn_raw_buffer_load_b32(rw, pfw, ((PU >> 1) & 1) * D * D * 2 + (PU & 1) * 32768 + (CO ^ (PU & 4)) * 65536, 0);
      }
      if constexpr (PV >= 0) { if (k == 20) PIN(pfr[PV]); }
      if (HS > RING && G == HS - HD && k == 12) inval();
      if (HS > RING && G == HS - HD && k == 14) hr = __hip_atomic_load(min_, RLX_AGENT);
      if (G == HS && k == 28) {
        hch = (unsigned)(hr >> 32) + 1u;
        __hip_atomic_store(mout, (hr << 32) | hch, RLX_AGENT);
      }
      if (G == 5 && k == 30) { PIN(cpf); PIN(pe); PIN(pf); }
      if (G == HG && k == 20) hr = __hip_atomic_load(min_, RLX_AGENT);
      if (G == TS - 2 && k == 26) { ho = (hr << 32) | hch; PIN(ho); }
      if (G == TS - 2 && k == 28) __hip_atomic_store(mout, ho, RLX_AGENT);
      if (G == TS - 2 && k == 12) bi4 = *(const GAS u32x2*)(bias + po);
      if (G == TS - 2 && k == 16) ga4 = *(const GAS u32x2*)(gamma + po);
      if (G == TS - 2 && k == 20) be4 = *(const GAS u32x2*)(beta + po);
      if constexpr (G >= SU) {
        SCHED();
        sfor<SC.v[(G - SU) * 32 + k], SC.v[(G - SU) * 32 + k + 1]>([&](auto UC) {
          constexpr int o = SL.v[decltype(UC)::value + 1];
          if ((o & 31) == 16) PIN(raw[o >> 6][3]);
          else shuf(o >> 6, (o >> 5) & 1, o & 15);
        });
      }
    });
  };
  sfor<1, TS>(step);

  f32x4 p[2][4];
  f32x2 d[4][4], S, Q, pA, pB;
  float piv, pvb[4], z, z2;
  u32x3 q[4];
  unsigned long long hbad;
  float scl[4], shf[4];
  u32x2 o[8];
  f32x2 tt[4];
  auto stA = [&](int a, int i) { *(LDS_AS float*)(lds + aa + i * API + ((a & 7) * 4 + (a >> 3)) * 256) = acc[a & 7][a >> 3][i]; };
  auto rdP = [&](int e, int n) { p[n >> 2][n & 3] = *(const LDS_AS f32x4*)(lds + ((n & 2) ? la1 : la0) + (n & 1) * PREG + (n >> 2) * 4096 + e * 256); };
  auto rdZ = [&](int e) { piv = *(const LDS_AS float*)(lds + lq + e * 256); };
  auto sumP = [&](int e, int h) {
    d[e][2 * h] = (lo2(p[h][0]) + lo2(p[h][1])) + (lo2(p[h][2]) + lo2(p[h][3]));
    d[e][2 * h + 1] = (hi2(p[h][0]) + hi2(p[h][1])) + (hi2(p[h][2]) + hi2(p[h][3]));
  };
  auto stat = [&](int e, int h) {
    f32x2 &d0 = d[e][2 * h], &d1 = d[e][2 * h + 1];
    d0 = fma2(d0, h ? mB : mA, -(h ? pB : pA));
    d1 = fma2(d1, f32x2{mB[1], mB[1]}, -f32x2{pB[1], pB[1]});
    S = h ? (S + d0) + d1 : d0 + d1;
    Q = fma2(d1, d1, h ? fma2(d0, d0, Q) : d0 * d0);
    if (h) { z = S[0] + S[1]; z2 = Q[0] + Q[1]; }
  };
  auto pub = [&](int e, int t) {
    if (t == 0 || t == 6) { auto r = __builtin_amdgcn_permlane32_swap(fu(z), fu(z2), false, false); z = uf(r[0]); z2 = uf(r[1]); }
    if (t == 1) z += z2;
    if (t == 2) z += dpp<0x128>(z);
    if (t == 3) z += dpp<0x124>(z);
    if (t == 4) { auto r = __builtin_amdgcn_permlane16_swap(fu(z), fu(z2), false, false); z = uf(r[0]); z2 = uf(r[1]); }
    if (t == 5) z = uf(min(fu(z + z2), INVALID - 1));
    if (t == 7) __builtin_amdgcn_raw_buffer_store_b96(u32x3{fu(z), fu(pvb[e]), fu(z2)}, rg, gs + e * GST, 0, 16);
    else PIN(z);
  };
  auto ldG = [&](int e) {
    asm volatile("buffer_load_dwordx3 %0, %1, %2, 0 offen offset:%3 sc1" : "=v"(q[e]) : "v"(go), "s"(rg), "n"(e * GST) : "memory");
  };
  auto bad = [&](int e, int n) {
    asm volatile("s_waitcnt vmcnt(%1)" : "+v"(q[e]) : "n"(n) : "memory");
    return __ballot(max(max(q[e][0], q[e][1]), q[e][2]) == INVALID);
  };
  auto poll = [&](int e, int n) {
    if (bad(e, n)) {
#pragma nounroll
      for (int sp = 0; sp < SPIN_CAP; ++sp) {
        __builtin_amdgcn_s_sleep(SLP);
        ldG(e);
        if (!bad(e, 0)) break;
      }
      if (e == 1 && !EP) { PIN(q[2]); ldG(2); }
    }
  };
  auto coef = [&](int e) {
    const u32x3 v = q[e];
    const float dl = uf(v[1]) - pvb[e];
    float a1 = __builtin_fmaf(nrow, dl, uf(v[0])), a2 = __builtin_fmaf(dl, uf(v[0]) + a1, uf(v[2]));
    sum16(a1, a2);
    const float dm = a1 * (1.0f / (float)L);
    scl[e] = __builtin_amdgcn_rsqf(__builtin_fmaf(-dm, dm, __builtin_fmaf(a2, 1.0f / (float)L, 1e-5f))) * bfel(ga4, e);
    shf[e] = __builtin_fmaf(-dm, scl[e], bfel(be4, e));
  };
  auto gelu = [&](int e, int mk, int ph = 3) {
#pragma unroll
    for (int i = 0; i < 4; ++i) {
      if (!((mk >> i) & 1)) continue;
      if (ph & 1) {
        const f32x2 u = d[e][i] = d[e][i] * scl[e] + shf[e];
        tt[i] = u * (u * u * -0.1029432397f - 2.3022081985f);
        if (ph == 1) PIN(tt[i]);
      }
      if (ph & 2) {
        const f32x2 r = f32x2{__builtin_amdgcn_exp2f(tt[i][0]), __builtin_amdgcn_exp2f(tt[i][1])} + 1.0f;
        d[e][i] *= f32x2{__builtin_amdgcn_rcpf(r[0]), __builtin_amdgcn_rcpf(r[1])};
      }
    }
#pragma unroll
    for (int i = 0; i < 8; ++i) {
      if (!((mk >> (i >> 1)) & 1) || !(ph & 2)) continue;
      if (e & 1) o[i][e >> 1] = __builtin_bit_cast(unsigned, bf16x2{(__bf16)d[e - 1][i >> 1][i & 1], (__bf16)d[e][i >> 1][i & 1]});
      if (e == 3 && TR) *(LDS_AS u32x2*)(lds + oz + (i >> 2) * 512 + (i & 3) * 1024) = o[i];
      else if (e == 3) __builtin_amdgcn_raw_buffer_store_b64(o[i], ro, i == 0 ? oz : i == 4 ? oy : oo, (i >> 2) * 4096 + (i & 3) * 8192, OAUX);
      else if (e == 1) PIN(o[i][0]);
      else PIN(d[e][i >> 1]);
    }
  };
  constexpr int NS = 60 - TS - NX;
  static_assert(TC.v[130] >= 12 * NS && TC.v[194] >= 16 * NS, "");
  auto fin = [&](auto AC) {
    constexpr int a = decltype(AC)::value, e = a >> 3, m = a & 7, r = a < 9 ? 0 : (a - 9) >> 3;
    sfor<0, 8>([&](auto JC) {
      constexpr int j = decltype(JC)::value, s = TS + ((a & 1) ? 7 - j : j), f = a < 9 || (j >= 2 && j <= 5) ? -1 : ((a - 9) & 7) * 4 + (j & 3);
      constexpr int fp = pasof(s) & 1, fi = m + posof(s), u0 = TC.v[8 * a + j], u1 = TC.v[8 * a + j + 1];
      SCHED();
      mfma<(s >= FAS)>(acc[m][e], B[s][e], F[fp][fi]);
      SCHED();
      if constexpr (a == 0 && j < 6) F[1][8 + j] = ldF(3, 8 + j);
      if (a == 1 && j == 7) { if (59 >= FAS) asm volatile("" :: "a"(F[1][13])); else PIN(F[1][13]); }
      if (a > 0 && j >= 2 && j <= 5) stA((a + 31) & 31, j - 2);
      if (f == 1 || (r == 2 && f == 16)) lds_barrier();
      if (f == 2) rdZ(r);
      if (f >= 3 && f <= 10) rdP(r, f - 3);
      if (r == 2 && f == 18) rdZ(3);
      if (r == 2 && (f == 19 || f == 20 || f == 21 || f == 24)) rdP(3, f == 24 ? 3 : f - 19);
      if (a == HA && j == 1) hr = __hip_atomic_load(min_, RLX_AGENT);
      if (a == 24 && j == 7) ldG(0);
      if (f == 26) pub(r, 7);
      SCHED();
      if (a == 24 && j == 6) { hbad = __ballot((unsigned)hr != (unsigned)(ho >> 32)); asm volatile("" :: "s"(hbad)); }
      if (a == 25 && j == 0) { hbad |= __ballot((unsigned)(hr >> 32) != hch); asm volatile("" :: "s"(hbad)); }
      if (f == 14) PIN(p[1][3]);
      if (f == 7 || (r == 2 && f == 24)) PIN(piv);
      if (f == 8 || (r == 2 && f == 26)) { piv += dpp<0x128>(piv); PIN(piv); }
      if (f == 9) { pvb[r] = bfel(bi4, r); PIN(pvb[r]); }
      if (f == 10) { piv += dpp<0x124>(piv); PIN(piv); }
      if (f == 11) { pvb[r] = uf(min(fu(pvb[r] + piv), INVALID - 1)); PIN(pvb[r]); }
      if (r == 2 && f == 20) { pvb[3] = bfel(bi4, 3); PIN(pvb[3]); }
      if (f == 15) {
        pA = mA * piv; pB = mB * piv;
        sumP(r, 0); sumP(r, 1); stat(r, 0); stat(r, 1);
        PIN(z); PIN(z2);
      }
      if (f == 17 || f == 18) pub(r, f - 17);
      if (f >= 21 && f <= 23) pub(r, f - 18);
      if (f == 25) pub(r, 6);
      SCHED();
#pragma unroll
      for (int u = u0; u < u1; ++u) {
        if (u < 12 * NS) shuf(TS + NX + u / 12, 1, u % 12);
        else if (u < 16 * NS) shuf(TS + NX + (u - 12 * NS) / 4, 1, 12 + (u & 3));
      }
      SCHED();
      if (f == 19) pub(r, 2);
    });
  };
  sfor<0, 32>(fin);
  STAMP(2);
  SCHED();
  ldG(1);
  SCHED();
  sumP(3, 0);
  piv += dpp<0x124>(piv);
  PIN(piv); PIN(d[3][0]); PIN(d[3][1]);
  SCHED();
#pragma unroll
  for (int i = 0; i < 4; ++i) { stA(31, i); SCHED(); }
  pA = mA * piv; pB = mB * piv;
  stat(3, 0);
  pvb[3] = uf(min(fu(pvb[3] + piv), INVALID - 1));
  hbad |= bad(0, 2);
  asm volatile("" :: "s"(hbad), "v"(pvb[3]));
  PIN(S); PIN(Q);
  SCHED();
  lds_barrier();
#pragma unroll
  for (int i = 4; i < 8; ++i) rdP(3, i);
  SCHED();
  coef(0);
  PIN(scl[0]); PIN(shf[0]);
  SCHED();
  sumP(3, 1); stat(3, 1);
  STAMP(3);
#pragma unroll
  for (int t = 0; t < 8; ++t) pub(3, t);
  SCHED();
  STAMP(7);
  if (__builtin_expect(hbad != 0, 0)) {
    unsigned sent = (unsigned)(ho >> 32);
    asm volatile("s_waitcnt vmcnt(0)" :: "v"(q[1]) : "memory");
#pragma nounroll
    for (int sp = 0; sp < SPIN_CAP; ++sp) {
      if (__any((unsigned)hr != sent)) { __hip_atomic_store(mout, (hr << 32) | hch, RLX_AGENT); sent = (unsigned)hr; }
      if (__all((unsigned)(hr >> 32) == hch)) break;
      __builtin_amdgcn_s_sleep(1);
      hr = __hip_atomic_load(min_, RLX_AGENT);
    }
    asm volatile("s_waitcnt vmcnt(0)" :: "v"(hr) : "memory");
    ldG(0); ldG(1);
    poll(0, 0);
    coef(0);
    asm volatile(".Lconv_code_end:" :: "v"(scl[0]), "v"(shf[0]));
  }
  if (EP) {
    poll(1, 1);
    ldG(2);
    SCHED();
    coef(1);
    gelu(0, 15);
    PIN(scl[1]); PIN(shf[1]);
    gelu(1, 15, 1);
    SCHED();
    poll(2, 0);
    if (EP == 1) ldG(3);
    SCHED();
    coef(2);
    gelu(1, 15, 2);
    PIN(scl[2]); PIN(shf[2]);
    SCHED();
    if (EP == 2) ldG(3);
    SCHED();
    gelu(2, 15);
  } else {
  gelu(0, 15);
  SCHED();
  ldG(2);
  XST(0);
  poll(1, 2);
  XST(1);
  coef(1);
  gelu(1, 15);
  SCHED();
  XST(2);
  poll(2, 0);
  ldG(3);
  XST(3);
  SCHED();
  coef(2);
  gelu(2, 15);
  }
  XST(4);
  poll(3, 0);
  XST(5);
  coef(3);
  STAMP(4);
  static_assert(CT && !SHT, "");
  u32x4 t[4];
  auto rdT = [&](int i) { t[i] = *(const LDS_AS u32x4*)(lds + oy + i * 1024); };
  auto stT = [&](int i) { __builtin_amdgcn_raw_buffer_store_b128(t[i], ro, oo, i * 8192, OAUX); };
  gelu(3, 5);
  SCHED();
  if (TR == 2) {
    gelu(3, 10, 1);
    SCHED();
    lds_barrier();
    rdT(0); rdT(1);
    SCHED();
    gelu(3, 10, 2);
    SCHED();
    stT(0); stT(1);
    lds_barrier();
    rdT(2); rdT(3);
    stT(2); stT(3);
  } else {
    gelu(3, 10);
    lds_barrier();
    rdT(0); rdT(1); rdT(2); rdT(3);
    stT(0); stT(1); stT(2); stT(3);
  }
  STAMP(5);
}

extern "C" void kernel_launch(void* const* d_in, const int* in_sizes, int n_in,
                              void* d_out, int out_size, void* d_ws, size_t ws_size,
                              hipStream_t stream) {
  const __bf16* x = (const __bf16*)d_in[0];
  const __bf16* w = (const __bf16*)d_in[1];
  const __bf16* bias = (const __bf16*)d_in[2];
  const __bf16* gamma = (const __bf16*)d_in[3];
  const __bf16* beta = (const __bf16*)d_in[4];
  unsigned long long* gran = (unsigned long long*)d_ws;
  unsigned long long* stamps = (unsigned long long*)((char*)d_ws + (size_t)32 * 1024 * 1024);
  const unsigned ON = L * D * 2;
  const unsigned F = 0x00020000u, XN = (2 * L + KT - 2) * D * 2;
  conv_gn_gelu_kernel<<<256, 256, 0, stream>>>(w, KT * D * D * 2, F, x, XN, F, x + T * D, XN, F, d_out, ON, F, (__bf16*)d_out + L * D, ON, F,
                                               gran, GRAN_BYTES, F, bias, gamma, beta, stamps);
}
```

```cpp
#include <hip/hip_runtime.h>
#include <type_traits>
#include <utility>

typedef __bf16 bf16x2 __attribute__((ext_vector_type(2)));
typedef __bf16 bf16x4 __attribute__((ext_vector_type(4)));
typedef __bf16 bf16x8 __attribute__((ext_vector_type(8)));
typedef float f32x4 __attribute__((ext_vector_type(4)));
typedef float f32x2 __attribute__((ext_vector_type(2)));
typedef unsigned u32x2 __attribute__((ext_vector_type(2)));
typedef unsigned u32x3 __attribute__((ext_vector_type(3)));
typedef unsigned u32x4 __attribute__((ext_vector_type(4)));
#define LDS_AS __attribute__((address_space(3)))
#define GAS __attribute__((address_space(1)))
#define DI __device__ __forceinline__

constexpr int T = 4000, D = 512, KT = 15, L = 1993;
constexpr int XPL = 544, XRES = 4 * XPL, XIMG = 8 * XRES, WREG = 2 * XIMG;
constexpr int LDS_BYTES = 4 * WREG;
constexpr int API = 8256, PREG = WREG + 16;
constexpr int SPIN_CAP = 1 << 20;
constexpr unsigned INVALID = 0xFFFFFFFFu;
constexpr int GST = 128;
constexpr int GRAN_BYTES = 4096 * GST;

#define RLX_AGENT __ATOMIC_RELAXED, __HIP_MEMORY_SCOPE_AGENT
#define SCHED() __builtin_amdgcn_sched_barrier(0)
#define FAS 46
#define PF1 6
#define PF2 20
#define PF3 36
#define GB 43
#define RB 4
#define SLP 1
#define HG 46
#define HA 12
#define OAUX 18
#define NX 0
constexpr int TS = 52;
constexpr int SU = 42;
#define RING 3
#define NPRE 10
#define NQ 4
#define HS 11
#define HD 4
#define XD 3
#define CO 4
#define QW 1
constexpr unsigned woff(int S) {
  return S < 16 ? (unsigned)((2 * (S & 7)) * (D * D * 2) + (S >> 3) * 32768)
                : (unsigned)((2 * ((S - 16) % 7) + 1) * (D * D * 2) + ((S - 16) / 7) * 32768);
}
constexpr int pasof(int G) { return G % 30 < 8 ? 0 : G % 30 < 16 ? 1 : G % 30 < 23 ? 2 : 3; }
constexpr int posof(int G) { return G % 30 - (pasof(G) == 0 ? 0 : pasof(G) == 1 ? 8 : pasof(G) == 2 ? 16 : 23); }
constexpr int NPF = 3, PFP[3] = {PF1, PF2, PF3};
constexpr int pfu(int G) {
  for (int i = 0; i < NPF; ++i)
    if (PFP[i] == G) return 2 * i + 2;
  return -1;
}
constexpr unsigned long long qmask() {
  unsigned long long m = 0;
  for (int i = 0; i < NPF; ++i) m |= QW ? 2ull << PFP[i] : 0ull;
  return m;
}
constexpr unsigned long long QM = qmask();
constexpr bool quiet(int G) { return (QM >> G) & 1; }
constexpr int xst(int im, int p) {
  const int a = im == 0 ? XD : im == 1 ? 16 : 30, z = im == 0 ? 15 : im == 1 ? 29 : 45;
  int n = 0;
  for (int g = a; g <= z; ++g) n += !quiet(g - XD);
  int i = (p * (n - 1) + 8) / 16;
  for (int g = a; g <= z; ++g)
    if (!quiet(g - XD) && i-- == 0) return g;
  return -1;
}
constexpr int xq(int G, int n) {
  for (int im = 0; im < 3; ++im)
    for (int p = 0; p < 17; ++p)
      if (xst(im, p) == G && n-- == 0) return im * 32 + p;
  return -1;
}
constexpr int wcnt(int G) {
  int n = 4 * (G + RING + 1) + (G > GB ? RB * (G - GB) : 0);
  for (int i = 0; i < NPF; ++i) {
    const int d = G - 1 - PFP[i];
    n += d <= 0 ? (4 * QW + d > 0 ? 4 * QW + d : 0) : (4 * QW - 4 * d > 0 ? 4 * QW - 4 * d : 0);
  }
  return n < 240 ? n : 240;
}
constexpr unsigned wmask(int G) {
  int wn = wcnt(G + 1) - wcnt(G);
  const int pos[8] = {14, 18, 22, 26, xq(G + XD, 0) < 0 ? 12 : -1, xq(G + XD, 1) < 0 ? 28 : -1, 30, 24};
  unsigned m = 0;
  for (int i = 0; i < 8; ++i)
    if (pos[i] >= 0 && wn > 0) { m |= 1u << pos[i]; --wn; }
  return m;
}
constexpr int fq(int G, int k) {
  const int PS = pasof(G), J = posof(G), NT = PS < 2 ? 8 : 7, JN = J - NT + 4 + (PS == 2);
  if (G == 1) return k == 0 ? 9 : k == 2 ? 10 : -1;
  if (J == 0 && k == 0) return 8;
  if (k == (J == 0 ? 2 : 0) && J + 2 < NT) return 9 + J;
  if ((k == 2 || k == 4) && JN >= 0 && JN < 4) return 16 + 2 * JN + (k == 4);
  return -1;
}
struct Tab { int v[512]; };
constexpr Tab slist() {
  Tab t{};
  int n = 0;
  for (int h = 0; h < 2 * (TS - SU) + NX + 58 - TS; ++h) {
    const int x = h - 2 * (TS - SU), G = SU + (h >> 1);
    const int s = x < 0 ? ((G + 1) & ~1) + (h & 1) : x < NX ? TS + x : TS + 2 + x - NX, j = x < 0 ? (G + 1) & 1 : x < NX;
    if (!j) t.v[++n] = s * 64 + 16;
    for (int i = 0; i < 16; ++i) t.v[++n] = s * 64 + j * 32 + i;
  }
  t.v[0] = n;
  return t;
}
constexpr Tab SL = slist();
constexpr Tab scum() {
  Tab t{};
  int n = SL.v[0];
  t.v[(TS - SU) * 32] = n;
  for (int G = TS - 1; G >= SU; --G) {
    const unsigned wm = wmask(G) | (xq(G, 0) >= 0 ? 0x154u : 0u) | (xq(G, 1) >= 0 ? 0x1110000u : 0u) | (wmask(G) ? 0x400u : 0u) |
                        (G == TS - 2 ? 0x14111000u : 0u) | (G == HG ? 1u << 20 : 0u) | (xq(G + XD, 0) >= 0 ? 1u << 12 : 0u) | (xq(G + XD, 1) >= 0 ? 1u << 28 : 0u);
    for (int k = 31; k >= 0; --k) {
      n -= (k & 1) ? 2 : ((wm >> k) & 1) ? 0 : 2 - (fq(G, k) >= 0) - (k == (posof(G) == 0 ? 8 : 30));
      const int o = (G - SU) * 33 - (G & 1) + (k & ~1) + ((G & 1) ? (k > 0) + (k > 16) : 0);
      if (n < o) n = o;
      t.v[(G - SU) * 32 + k] = n;
    }
  }
  return t;
}
constexpr int flast(int G) { return fq(G, 4) >= 0 ? fq(G, 4) : fq(G, 2) >= 0 ? fq(G, 2) : fq(G, 0); }
constexpr int tocc(int a, int j) {
  if (a == 0) return j < 6;
  if (j >= 2 && j <= 5) return 2;
  if (a < 9) return a == 1 && j == 7;
  if (a == HA && j == 1) return 2;
  const int occ[32] = {0, 2, 1, 1, 1, 1, 1, 2, 2, 2, 2, 2, 0, 0, 1, 2, 0, 2, 1, 1, 0, 1, 2, 2, 0, 2, 1};
  return occ[((a - 9) & 7) * 4 + (j & 3)];
}
constexpr Tab tcum() {
  Tab t{};
  for (int u = 0; u < 256; ++u) t.v[u + 1] = t.v[u] + 2 - tocc(u >> 3, u & 7);
  return t;
}
constexpr Tab SC = scum(), TC = tcum();
static_assert(SC.v[0] == 0 && !(SU & 1), "");
template <int A, int... I, class FT> DI void sfor_(std::integer_sequence<int, I...>, FT&& f) { (f(std::integral_constant<int, A + I>{}), ...); }
template <int A, int Z, class FT> DI void sfor(FT&& f) { sfor_<A>(std::make_integer_sequence<int, Z - A>{}, f); }

DI unsigned xsel(unsigned oth, unsigned own) {
  unsigned r;
  register unsigned long long m asm("vcc") = 0xFF00FF00FF00FF00ull;
  asm volatile("v_cndmask_b32_dpp %0, %1, %2, vcc row_ror:8 row_mask:0xf bank_mask:0xf" : "=v"(r) : "v"(oth), "v"(own), "s"(m));
  return r;
}
template <bool FA>
DI void mfma(f32x4& acc, const u32x4& a, const bf16x8& b) {
  if (FA) asm volatile("v_mfma_f32_16x16x32_bf16 %0, %1, %2, %0" : "+a"(acc) : "v"(a), "a"(b));
  else asm volatile("v_mfma_f32_16x16x32_bf16 %0, %1, %2, %0" : "+a"(acc) : "v"(a), "v"(b));
}
DI void mfma0(f32x4& acc, const u32x4& a, const bf16x8& b) {
  asm volatile("v_mfma_f32_16x16x32_bf16 %0, %1, %2, 0" : "=a"(acc) : "v"(a), "v"(b));
}
DI void setm0(unsigned v) { asm volatile("s_mov_b32 m0, %0" :: "s"(v) : "memory"); }
DI void st_tid(unsigned v, int off) { asm volatile("ds_write_addtid_b32 %0 offset:%1" :: "v"(v), "n"(off) : "memory"); }
DI void lds_barrier() {
  asm volatile("" ::: "memory");
  __builtin_amdgcn_s_waitcnt(0xC07F);
  __builtin_amdgcn_s_barrier();
  asm volatile("" ::: "memory");
}
DI unsigned fu(float v) { return __builtin_bit_cast(unsigned, v); }
DI float uf(unsigned v) { return __builtin_bit_cast(float, v); }
DI f32x2 lo2(const f32x4& v) { return f32x2{v[0], v[1]}; }
DI f32x2 hi2(const f32x4& v) { return f32x2{v[2], v[3]}; }
DI f32x2 fma2(f32x2 a, f32x2 b, f32x2 c) { return __builtin_elementwise_fma(a, b, c); }
DI float bfel(const u32x2& v, int e) { return uf((e & 1) ? v[e >> 1] & 0xFFFF0000u : v[e >> 1] << 16); }
#define PIN(x) asm volatile("" :: "v"(x))
template <int CTRL>
DI float dpp(float v) { return __builtin_bit_cast(float, __builtin_amdgcn_update_dpp(0, __builtin_bit_cast(int, v), CTRL, 0xF, 0xF, true)); }

DI void sum16(float& a, float& b) {
  auto p = __builtin_amdgcn_permlane32_swap(fu(a), fu(b), false, false);
  float v = uf(p[0]) + uf(p[1]);
  v += dpp<0x128>(v);
  v += dpp<0x124>(v);
  auto r = __builtin_amdgcn_permlane16_swap(fu(v), fu(v), false, false);
  v = uf(r[0]) + uf(r[1]);
  auto q = __builtin_amdgcn_permlane32_swap(fu(v), fu(v), false, false);
  a = uf(q[0]); b = uf(q[1]);
}

#define SST(k) do {} while (0)
#define XST(k) do {} while (0)
#define STAMP(k) do {} while (0)

__global__ __launch_bounds__(256, 1)
void conv_gn_gelu_kernel(const void* w, unsigned wn, unsigned wf, const void* x0, unsigned x0n, unsigned x0f, const void* x1, unsigned x1n, unsigned x1f,
                         void* o0, unsigned o0n, unsigned o0f, void* o1, unsigned o1n, unsigned o1f, unsigned long long* gran, unsigned gn, unsigned gf,
                         const void* bi, const void* ga, const void* be,
                         unsigned long long* __restrict__ stamps) {
  typedef const __amdgpu_buffer_rsrc_t __attribute__((address_space(4)))* KA;
  const KA ka = (KA)__builtin_amdgcn_kernarg_segment_ptr();
  const __amdgpu_buffer_rsrc_t rw = ka[0], rx = ka[1 + ((blockIdx.x >> 1) & 1)], rg = ka[5];
  typedef char* const __attribute__((address_space(4)))* KP;
  const char *bias = ((KP)ka)[12], *gamma = ((KP)ka)[13], *beta = ((KP)ka)[14];
  GAS unsigned long long* mbase = (GAS unsigned long long*)((KP)ka)[10];
  SCHED();
  __shared__ __attribute__((aligned(1024))) char smem[LDS_BYTES];
  LDS_AS char* const lds = (LDS_AS char*)smem;
  const int tid = threadIdx.x, lane = tid & 63;
  const int wv = __builtin_amdgcn_readfirstlane(tid >> 6);
  const int bid = blockIdx.x;
  const int xcd = bid & 7, idx = bid >> 3;
  const int b = (xcd >> 1) & 1, mt = idx & 15, nt = (xcd & 1) * 4 + (xcd >> 2) * 2 + (idx >> 4);
  const int l0 = mt * 128, n0 = nt * 64;
  const int c = lane & 15, g = lane >> 4;
  STAMP(0);

  auto ldC = [&](int skip) {
    unsigned long long pc;
    unsigned len;
    asm volatile("s_getpc_b64 %0\n\ts_mov_b32 %1, .Lconv_code_end-." : "=s"(pc), "=s"(len));
    const __amdgpu_buffer_rsrc_t rc = __builtin_amdgcn_make_buffer_rsrc((void*)pc, 0, len, 0x00020000);
    return __builtin_amdgcn_raw_buffer_load_b32(rc, (unsigned)((lane * 128 + idx * 4 + wv) * 128) | (skip == 1 && lane ? 0x80000000u : 0u), skip == 2 ? 12288 : 0, 0);
  };
  const unsigned cp0 = ldC(1);
  SCHED();
  unsigned wo = (unsigned)((2 * g + 16 * (c >> 3)) * 1024 + n0 * 2 + (c & 7) * 16 + wv * 65536), wo2 = wo + 8192;
  unsigned xo0 = (unsigned)((2 * l0 + 16 * (lane >> 3)) * (D * 2) + ((lane ^ (lane >> 1)) & 4) * 16 + (lane & 3) * 16 + (wv + CO) * 128);
  asm volatile("" : "+v"(wo), "+v"(wo2), "+v"(xo0));
  SCHED();
  const int grp = b * 8 + nt;
  const int gbase = (grp * 4 + wv) * 64 * GST;
  const int ei = lane & 3, cq = c >> 2, ch0 = n0 + 16 * wv + 4 * ei;
  const int lb = (2 * (wv & 1) + (ei >> 1)) * API + ((wv >> 1) + 2 * (ei & 1)) * 64;
  unsigned aa = (unsigned)(wv * PREG + lane * 4);
  unsigned la0 = (unsigned)(lb + g * 1024 + cq * 16), la1 = la0 + 2 * PREG;
  unsigned lq = (unsigned)(lb + cq * PREG + (ei & 1) * 4);
  unsigned gs = lane >= 4 ? 0x80000000u : (unsigned)(gbase + mt * 4 * GST + ei * 16);
  unsigned go = (unsigned)(gbase + (lane >> 2) * 4 * GST + ei * 16);
  asm volatile("" : "+v"(aa), "+v"(la0), "+v"(la1), "+v"(lq), "+v"(gs), "+v"(go));

  SCHED();

  GAS unsigned long long *min_, *mout;

  u32x4 raw[60][4];
  unsigned so[60];
  auto ldW = [&](int n) {
    raw[n >> 2][n & 3] = __builtin_bit_cast(u32x4, __builtin_amdgcn_raw_buffer_load_b128(rw, ((n & 2) ? wo2 : wo) + (n & 1) * 1024, so[n >> 2], 0));
  };
#pragma unroll
  for (int i = 0; i < RING + 2; ++i) so[i] = woff(i) + CO * 65536;
  SCHED();
#pragma unroll
  for (int i = 0; i < 4; ++i) ldW(i);
  SCHED();
  SCHED();
  SST(0);

  unsigned xo1 = (unsigned)((2 * l0 + 256 + 2 * (lane >> 3)) * (D * 2) + (lane & 7) * 16 + (wv + CO) * 128);
  const unsigned xa1 = (unsigned)(wv * WREG + (lane >> 3) * XRES + 512 + (lane & 7) * 4);
  auto ldX = [&](int pc) -> u32x4 {
    return __builtin_bit_cast(u32x4, __builtin_amdgcn_raw_buffer_load_b128(rx, pc == 16 ? xo1 : xo0, (pc & 14) * 1024 + (pc & 1) * 131072, 0));
  };
  auto stX = [&](int p, int pc, const u32x4& v, int d) {
    if (pc == 16) *(LDS_AS unsigned*)(lds + xa1 + p * XIMG + d * XPL) = v[d];
    else st_tid(v[d], p * XIMG + (pc >> 1) * XRES + d * XPL + (pc & 1) * 256);
  };
  u32x4 xs[32];
  unsigned long long hr;
  auto inval = [&]() { __builtin_amdgcn_raw_buffer_store_b64(u32x2{INVALID, INVALID}, rg, lane * 8, gbase + mt * 4 * GST, 16); };
  auto ldS = [&](int n) {
    if (n < 16) {
      xs[n & 15] = ldX(n);
    }
    else if (n < 20) ldW(n - 12);
    else if (n == 20) xs[16] = ldX(16);
    else if (n < 21 + 4 * (RING - 1)) {
      ldW(n - 13);
    }
  };
#pragma unroll
  for (int i = 0; i < NPRE; ++i) { ldS(i); SCHED(); }
  SCHED();
  int b2 = b;
  asm volatile("" : "+s"(b2));
  __amdgpu_buffer_rsrc_t ro = ka[3 + b2];
  setm0(wv * WREG);

  unsigned pfr[8];
  const unsigned pfw = (unsigned)(((lane >> 2) * 2 * D + idx * 2 + ((lane >> 1) & 1)) * (D * 2) + (n0 & ~64) * 2 + (lane & 1) * 128 + wv * 65536);

  unsigned oz = (unsigned)(cq * (PREG + 16) + g * 128 + wv * 32 + ei * 8), oy = (unsigned)(wv * (PREG + 16) + lane * 16);
  unsigned oo = (unsigned)((l0 + 32 * wv + (lane >> 3)) * (D * 2) + n0 * 2 + (lane & 7) * 16);
  const int lim = L - l0 - 32 * cq - g;
  f32x2 mA = {lim > 0 ? 1.f : 0.f, lim > 8 ? 1.f : 0.f}, mB = {lim > 4 ? 1.f : 0.f, lim > 12 ? 1.f : 0.f};
  unsigned po = (unsigned)(ch0 * 2);
  float nrow = lane >= 60 ? (float)(L - 15 * 128) : 128.f;
  asm volatile("" : "+v"(po), "+v"(nrow), "+v"(oo), "+v"(oz), "+v"(oy), "+v"(mA), "+v"(mB));
  SCHED();
  mbase += GRAN_BYTES / 8 + (grp * 4 + wv) * 256;
  min_ = mbase + mt * 16 + c;
  mout = mbase + c * 16 + mt;
  asm volatile("" : "+s"(ro), "+s"(bias), "+s"(gamma), "+s"(beta));

  unsigned fa[2][2];
#pragma unroll
  for (int hi = 0; hi < 2; ++hi) {
    const int v = c + hi;
    fa[0][hi] = (unsigned)(wv * WREG + g * XPL + v * 32 + (v & 1) * 16);
    fa[1][hi] = fa[0][hi] ^ 16u;
  }
  auto ldF = [&](int pass, int q) -> bf16x8 {
    return *(LDS_AS bf16x8*)(lds + fa[pass & 1][q >> 3] + (pass >> 1) * XIMG + (q & 7) * XRES);
  };
  unsigned U[60][4][2], V[60][4][2];
  u32x4 B[60][4];
  auto mkU = [&](int s, int r, int j) { U[s][r][j] = xsel(raw[s][r][j], raw[s][r][2 + j]); };
  auto mkV = [&](int s, int r, int j) { V[s][r][j] = __builtin_amdgcn_update_dpp(raw[s][r][j], raw[s][r][2 + j], 0x128, 0xF, 0xC, false); };
  auto mkB = [&](int s, int e, int d) {
    const unsigned sel = (e & 1) ? 0x07060302u : 0x05040100u;
    B[s][e][d] = d < 2 ? __builtin_amdgcn_perm(V[s][2 * d + 1][e >> 1], V[s][2 * d][e >> 1], sel)
                       : __builtin_amdgcn_perm(U[s][2 * d - 3][e >> 1], U[s][2 * d - 4][e >> 1], sel);
  };

  f32x4 acc[8][4];
  bf16x8 F[2][15];
  SST(1);
  SCHED();
#pragma unroll
  for (int i = 0; i < 8; ++i) mkU(0, i >> 1, i & 1);
  SCHED();
  SST(2);
#pragma unroll
  for (int i = 0; i < 8; ++i) mkV(0, i >> 1, i & 1);
#pragma unroll
  for (int i = 0; i < 16; ++i) mkB(0, i >> 2, i & 3);
  SCHED();
#pragma unroll
  for (int q = 0; q <= 8; ++q) {
    if (q < 8) {
#pragma unroll
      for (int i = 0; i < 8; ++i) stX(0, 2 * q + (i >> 2), xs[2 * q + (i >> 2)], i & 3);
      F[0][q] = ldF(0, q);
      if (q == 0) SST(3);
      if (q == 3) SST(4);
      if (q == 7) SST(5);
    }
#pragma unroll
    for (int e = 0; e < 4; ++e) {
      SCHED();
      if (q > 0) mfma0(acc[q - 1][e], B[0][e], F[0][q - 1]);
      SCHED();
      if (e < NQ) ldS(NPRE + NQ * q + e);
    }
  }
  static_assert(NPRE + NQ * 9 >= 21 + 4 * (RING - 1), "");
  xo0 += 1024; xo1 += 1024;
  SCHED();
#pragma unroll
  for (int i = 0; i < 8; ++i) mkU(1, i >> 1, i & 1);
  SCHED();
  SST(6);
#pragma unroll
  for (int i = 0; i < 8; ++i) mkV(1, i >> 1, i & 1);
#pragma unroll
  for (int i = 0; i < 4; ++i) stX(0, 16, xs[16], i);
  F[0][8] = ldF(0, 8);
#pragma unroll
  for (int i = 0; i < 16; ++i) mkB(1, i >> 2, i & 3);
  PIN(cp0);
  SCHED();
#pragma unroll
  for (int i = 0; i < 4; ++i) ldW(wcnt(0) + i);
  xs[0] = ldX(0);
  if (xq(XD, 1) >= 0) xs[1] = ldX(1);
  pfr[0] = __builtin_amdgcn_raw_buffer_load_b32(rw, pfw, CO * 65536, 0);
  const unsigned cpf = ldC(2);
  u32x2 bi4, ga4, be4;
  SCHED();
  STAMP(1);
  unsigned hch;
  unsigned long long ho;

  auto shuf = [&](int s, int j, int t) {
    if (t >= 8) mkB(s, 2 * j + ((t >> 2) & 1), t & 3);
    else if (t & 1) mkV(s, t >> 1, j);
    else mkU(s, t >> 1, j);
  };
  auto step = [&](auto GC) {
    constexpr int G = decltype(GC)::value;
    constexpr int PS = pasof(G), J = posof(G);
    constexpr int W0 = xq(G, 0), W1 = xq(G, 1), L0 = xq(G + XD, 0), L1 = xq(G + XD, 1);
    constexpr int WA = wcnt(G), WN = wcnt(G + 1) - WA;
    constexpr unsigned WM = wmask(G);
    constexpr int PU = pfu(G), PV = pfu(G - 5);
    constexpr int G0 = G & ~1, N0 = (G + 1) & ~1, NJ = (G + 1) & 1;
    static_assert(xq(G, 2) < 0 && WN >= 0 && WN == __builtin_popcount(WM), "");
    static_assert(W0 < 0 || G < ((W0 >> 5) == 0 ? 12 : (W0 >> 5) == 1 ? 26 : 42) + ((W0 & 31) >> 2), "");
    static_assert(W1 < 0 || G < ((W1 >> 5) == 0 ? 12 : (W1 >> 5) == 1 ? 26 : 42) + ((W1 & 31) >> 2), "");
    static_assert(wcnt(TS - 1) == 240, "");

    sfor<0, 32>([&](auto KC) {
      constexpr int k = decltype(KC)::value, FQ = fq(G, k), FL = flast(G);
      constexpr int e = G == 1 ? k >> 3 : 2 * (G & 1) + (k >> 4), kk = (k & 16) ? 31 - k : k;
      constexpr int m = G == 1 ? k & 7 : kk >> 1, sj = G == 1 ? 1 : G0 + (kk & 1), fp = pasof(sj) & 1, fi = m + posof(sj);
      SCHED();
      mfma<(sj >= FAS)>(acc[m][e], B[sj][e], F[fp][fi]);
      SCHED();
      if ((k & 1) && G < SU) { shuf(N0 + (k >> 4), NJ, (k - 1) & 15); shuf(N0 + (k >> 4), NJ, k & 15); }
      if constexpr (FQ >= 0) F[(PS + (FQ >> 4)) & 1][FQ & 15] = ldF((PS + (FQ >> 4)) & 3, FQ & 15);
      if ((k == 0 || k == 16) && NJ == 0 && G < SU) PIN(raw[N0 + (k >> 4)][3]);
      if (k >= 2 && k <= 8 && !(k & 1) && W0 >= 0) stX((W0 >> 5) != 1, W0 & 31, xs[W0 & 31], (k - 2) >> 1);
      if ((k == 10 || k == 16 || k == 20 || k == 24) && W1 >= 0) stX((W1 >> 5) != 1, W1 & 31, xs[W1 & 31], k == 10 ? 0 : (k - 12) >> 2);
      if (k == 10) {
#pragma unroll
        for (int s = (WA + 3) >> 2; 4 * s < WA + WN; ++s) asm volatile("s_mov_b32 %0, %1" : "=s"(so[s]) : "n"(woff(s % 30) + (CO ^ (s / 30 * 4)) * 65536));
        if (L0 == 32) { xo0 ^= 1536; xo1 ^= 1536; }
        if (L0 == 64) { xo0 += 1024; xo1 += 1024; }
      }
      if (k == 12 && L0 >= 0) xs[L0 & 31] = ldX(L0 & 31);
      if (k == 28 && L1 >= 0) xs[L1 & 31] = ldX(L1 & 31);
      if ((WM >> k) & 1) ldW(WA + __builtin_popcount(WM & ((1u << k) - 1)));
      if constexpr (k == (J == 0 ? 8 : 30) && FL >= 0) {
        if (G - J + (FL >> 4) * (PS < 2 ? 8 : 7) >= FAS) asm volatile("" :: "a"(F[(PS + (FL >> 4)) & 1][FL & 15]));
        else PIN(F[(PS + (FL >> 4)) & 1][FL & 15]);
      }
      if constexpr (PU >= 0) {
        if (k == 30) pfr[PU] = __builtin_amdgcn_raw_buffer_load_b32(rw, pfw, ((PU >> 1) & 1) * D * D * 2 + (CO ^ (PU & 4)) * 65536, 0);
      }
      if constexpr (PV >= 0) { if (k == 20) PIN(pfr[PV]); }
      if (G == HS - HD && k == 12) inval();
      if (G == HS - HD && k == 14) hr = __hip_atomic_load(min_, RLX_AGENT);
      if (G == HS && k == 28) {
        hch = (unsigned)(hr >> 32) + 1u;
        __hip_atomic_store(mout, (hr << 32) | hch, RLX_AGENT);
      }
      if (G == 5 && k == 30) { PIN(cpf); PIN(pfr[0]); }
      if (G == HG && k == 20) hr = __hip_atomic_load(min_, RLX_AGENT);
      if (G == TS - 2 && k == 26) { ho = (hr << 32) | hch; PIN(ho); }
      if (G == TS - 2 && k == 28) __hip_atomic_store(mout, ho, RLX_AGENT);
      if (G == TS - 2 && k == 12) bi4 = *(const GAS u32x2*)(bias + po);
      if (G == TS - 2 && k == 16) ga4 = *(const GAS u32x2*)(gamma + po);
      if (G == TS - 2 && k == 20) be4 = *(const GAS u32x2*)(beta + po);
      if constexpr (G >= SU) {
        SCHED();
        sfor<SC.v[(G - SU) * 32 + k], SC.v[(G - SU) * 32 + k + 1]>([&](auto UC) {
          constexpr int o = SL.v[decltype(UC)::value + 1];
          if ((o & 31) == 16) PIN(raw[o >> 6][3]);
          else shuf(o >> 6, (o >> 5) & 1, o & 15);
        });
      }
    });
  };
  sfor<1, TS>(step);

  f32x4 p[2][4];
  f32x2 d[4][4], S, Q, pA, pB;
  float piv, pvb[4], z, z2;
  u32x3 q[4];
  unsigned long long hbad;
  float scl[4], shf[4];
  u32x2 o[8];
  f32x2 tt[4];
  auto stA = [&](int a, int i) { *(LDS_AS float*)(lds + aa + i * API + ((a & 7) * 4 + (a >> 3)) * 256) = acc[a & 7][a >> 3][i]; };
  auto rdP = [&](int e, int n) { p[n >> 2][n & 3] = *(const LDS_AS f32x4*)(lds + ((n & 2) ? la1 : la0) + (n & 1) * PREG + (n >> 2) * 4096 + e * 256); };
  auto rdZ = [&](int e) { piv = *(const LDS_AS float*)(lds + lq + e * 256); };
  auto sumP = [&](int e, int h) {
    d[e][2 * h] = (lo2(p[h][0]) + lo2(p[h][1])) + (lo2(p[h][2]) + lo2(p[h][3]));
    d[e][2 * h + 1] = (hi2(p[h][0]) + hi2(p[h][1])) + (hi2(p[h][2]) + hi2(p[h][3]));
  };
  auto stat = [&](int e, int h) {
    f32x2 &d0 = d[e][2 * h], &d1 = d[e][2 * h + 1];
    d0 = fma2(d0, h ? mB : mA, -(h ? pB : pA));
    d1 = fma2(d1, f32x2{mB[1], mB[1]}, -f32x2{pB[1], pB[1]});
    S = h ? (S + d0) + d1 : d0 + d1;
    Q = fma2(d1, d1, h ? fma2(d0, d0, Q) : d0 * d0);
    if (h) { z = S[0] + S[1]; z2 = Q[0] + Q[1]; }
  };
  auto pub = [&](int e, int t) {
    if (t == 0 || t == 6) { auto r = __builtin_amdgcn_permlane32_swap(fu(z), fu(z2), false, false); z = uf(r[0]); z2 = uf(r[1]); }
    if (t == 1) z += z2;
    if (t == 2) z += dpp<0x128>(z);
    if (t == 3) z += dpp<0x124>(z);
    if (t == 4) { auto r = __builtin_amdgcn_permlane16_swap(fu(z), fu(z2), false, false); z = uf(r[0]); z2 = uf(r[1]); }
    if (t == 5) z = uf(min(fu(z + z2), INVALID - 1));
    if (t == 7) __builtin_amdgcn_raw_buffer_store_b96(u32x3{fu(z), fu(pvb[e]), fu(z2)}, rg, gs + e * GST, 0, 16);
    else PIN(z);
  };
  auto ldG = [&](int e) {
    asm volatile("buffer_load_dwordx3 %0, %1, %2, 0 offen offset:%3 sc1" : "=v"(q[e]) : "v"(go), "s"(rg), "n"(e * GST) : "memory");
  };
  auto bad = [&](int e, int n) {
    asm volatile("s_waitcnt vmcnt(%1)" : "+v"(q[e]) : "n"(n) : "memory");
    return __ballot(max(max(q[e][0], q[e][1]), q[e][2]) == INVALID);
  };
  auto poll = [&](int e, int n) {
    if (bad(e, n)) {
#pragma nounroll
      for (int sp = 0; sp < SPIN_CAP; ++sp) {
        __builtin_amdgcn_s_sleep(SLP);
        ldG(e);
        if (!bad(e, 0)) break;
      }
    }
  };
  auto coef = [&](int e) {
    const u32x3 v = q[e];
    const float dl = uf(v[1]) - pvb[e];
    float a1 = __builtin_fmaf(nrow, dl, uf(v[0])), a2 = __builtin_fmaf(dl, uf(v[0]) + a1, uf(v[2]));
    sum16(a1, a2);
    const float dm = a1 * (1.0f / (float)L);
    scl[e] = __builtin_amdgcn_rsqf(__builtin_fmaf(-dm, dm, __builtin_fmaf(a2, 1.0f / (float)L, 1e-5f))) * bfel(ga4, e);
    shf[e] = __builtin_fmaf(-dm, scl[e], bfel(be4, e));
  };
  auto gelu = [&](int e, int mk, int ph = 3) {
#pragma unroll
    for (int i = 0; i < 4; ++i) {
      if (!((mk >> i) & 1)) continue;
      if (ph & 1) {
        const f32x2 u = d[e][i] = d[e][i] * scl[e] + shf[e];
        tt[i] = u * (u * u * -0.1029432397f - 2.3022081985f);
        if (ph == 1) PIN(tt[i]);
      }
      if (ph & 2) {
        const f32x2 r = f32x2{__builtin_amdgcn_exp2f(tt[i][0]), __builtin_amdgcn_exp2f(tt[i][1])} + 1.0f;
        d[e][i] *= f32x2{__builtin_amdgcn_rcpf(r[0]), __builtin_amdgcn_rcpf(r[1])};
      }
    }
#pragma unroll
    for (int i = 0; i < 8; ++i) {
      if (!((mk >> (i >> 1)) & 1) || !(ph & 2)) continue;
      if (e & 1) o[i][e >> 1] = __builtin_bit_cast(unsigned, bf16x2{(__bf16)d[e - 1][i >> 1][i & 1], (__bf16)d[e][i >> 1][i & 1]});
      if (e == 3) *(LDS_AS u32x2*)(lds + oz + (i >> 2) * 512 + (i & 3) * 1024) = o[i];
      else if (e == 1) PIN(o[i][0]);
      else PIN(d[e][i >> 1]);
    }
  };
  constexpr int NS = 60 - TS - NX;
  static_assert(TC.v[130] >= 12 * NS && TC.v[194] >= 16 * NS, "");
  auto fin = [&](auto AC) {
    constexpr int a = decltype(AC)::value, e = a >> 3, m = a & 7, r = a < 9 ? 0 : (a - 9) >> 3;
    sfor<0, 8>([&](auto JC) {
      constexpr int j = decltype(JC)::value, s = TS + ((a & 1) ? 7 - j : j), f = a < 9 || (j >= 2 && j <= 5) ? -1 : ((a - 9) & 7) * 4 + (j & 3);
      constexpr int fp = pasof(s) & 1, fi = m + posof(s), u0 = TC.v[8 * a + j], u1 = TC.v[8 * a + j + 1];
      SCHED();
      mfma<(s >= FAS)>(acc[m][e], B[s][e], F[fp][fi]);
      SCHED();
      if constexpr (a == 0 && j < 6) F[1][8 + j] = ldF(3, 8 + j);
      if (a == 1 && j == 7) { if (59 >= FAS) asm volatile("" :: "a"(F[1][13])); else PIN(F[1][13]); }
      if (a > 0 && j >= 2 && j <= 5) stA((a + 31) & 31, j - 2);
      if (f == 1 || (r == 2 && f == 16)) lds_barrier();
      if (f == 2) rdZ(r);
      if (f >= 3 && f <= 10) rdP(r, f - 3);
      if (r == 2 && f == 18) rdZ(3);
      if (r == 2 && (f == 19 || f == 20 || f == 21 || f == 24)) rdP(3, f == 24 ? 3 : f - 19);
      if (a == HA && j == 1) hr = __hip_atomic_load(min_, RLX_AGENT);
      if (a == 24 && j == 7) ldG(0);
      if (f == 26) pub(r, 7);
      SCHED();
      if (a == 24 && j == 6) { hbad = __ballot((unsigned)hr != (unsigned)(ho >> 32)); asm volatile("" :: "s"(hbad)); }
      if (a == 25 && j == 0) { hbad |= __ballot((unsigned)(hr >> 32) != hch); asm volatile("" :: "s"(hbad)); }
      if (f == 14) PIN(p[1][3]);
      if (f == 7 || (r == 2 && f == 24)) PIN(piv);
      if (f == 8 || (r == 2 && f == 26)) { piv += dpp<0x128>(piv); PIN(piv); }
      if (f == 9) { pvb[r] = bfel(bi4, r); PIN(pvb[r]); }
      if (f == 10) { piv += dpp<0x124>(piv); PIN(piv); }
      if (f == 11) { pvb[r] = uf(min(fu(pvb[r] + piv), INVALID - 1)); PIN(pvb[r]); }
      if (r == 2 && f == 20) { pvb[3] = bfel(bi4, 3); PIN(pvb[3]); }
      if (f == 15) {
        pA = mA * piv; pB = mB * piv;
        sumP(r, 0); sumP(r, 1); stat(r, 0); stat(r, 1);
        PIN(z); PIN(z2);
      }
      if (f == 17 || f == 18) pub(r, f - 17);
      if (f >= 21 && f <= 23) pub(r, f - 18);
      if (f == 25) pub(r, 6);
      SCHED();
#pragma unroll
      for (int u = u0; u < u1; ++u) {
        if (u < 12 * NS) shuf(TS + NX + u / 12, 1, u % 12);
        else if (u < 16 * NS) shuf(TS + NX + (u - 12 * NS) / 4, 1, 12 + (u & 3));
      }
      SCHED();
      if (f == 19) pub(r, 2);
    });
  };
  sfor<0, 32>(fin);
  STAMP(2);
  SCHED();
  ldG(1);
  SCHED();
  sumP(3, 0);
  piv += dpp<0x124>(piv);
  PIN(piv); PIN(d[3][0]); PIN(d[3][1]);
  SCHED();
#pragma unroll
  for (int i = 0; i < 4; ++i) { stA(31, i); SCHED(); }
  pA = mA * piv; pB = mB * piv;
  stat(3, 0);
  pvb[3] = uf(min(fu(pvb[3] + piv), INVALID - 1));
  hbad |= bad(0, 2);
  asm volatile("" :: "s"(hbad), "v"(pvb[3]));
  PIN(S); PIN(Q);
  SCHED();
  lds_barrier();
#pragma unroll
  for (int i = 4; i < 8; ++i) rdP(3, i);
  SCHED();
  coef(0);
  PIN(scl[0]); PIN(shf[0]);
  SCHED();
  sumP(3, 1); stat(3, 1);
  STAMP(3);
#pragma unroll
  for (int t = 0; t < 8; ++t) pub(3, t);
  SCHED();
  STAMP(7);
  if (__builtin_expect(hbad != 0, 0)) {
    unsigned sent = (unsigned)(ho >> 32);
    asm volatile("s_waitcnt vmcnt(0)" :: "v"(q[1]) : "memory");
#pragma nounroll
    for (int sp = 0; sp < SPIN_CAP; ++sp) {
      if (__any((unsigned)hr != sent)) { __hip_atomic_store(mout, (hr << 32) | hch, RLX_AGENT); sent = (unsigned)hr; }
      if (__all((unsigned)(hr >> 32) == hch)) break;
      __builtin_amdgcn_s_sleep(1);
      hr = __hip_atomic_load(min_, RLX_AGENT);
    }
    asm volatile("s_waitcnt vmcnt(0)" :: "v"(hr) : "memory");
    ldG(0); ldG(1);
    poll(0, 0);
    coef(0);
    asm volatile(".Lconv_code_end:" :: "v"(scl[0]), "v"(shf[0]));
  }
  poll(1, 1);
  ldG(2);
  XST(0);
  SCHED();
  coef(1);
  gelu(0, 15);
  PIN(scl[1]); PIN(shf[1]);
  gelu(1, 15, 1);
  SCHED();
  XST(1);
  poll(2, 0);
  ldG(3);
  XST(2);
  SCHED();
  coef(2);
  gelu(1, 15, 2);
  PIN(scl[2]); PIN(shf[2]);
  SCHED();
  XST(3);
  gelu(2, 15);
  XST(4);
  poll(3, 0);
  XST(5);
  coef(3);
  STAMP(4);
  u32x4 t[4];
  auto rdT = [&](int i) { t[i] = *(const LDS_AS u32x4*)(lds + oy + i * 1024); };
  auto stT = [&](int i) { __builtin_amdgcn_raw_buffer_store_b128(t[i], ro, oo, i * 8192, OAUX); };
  gelu(3, 5);
  SCHED();
  gelu(3, 10);
  lds_barrier();
  rdT(0); rdT(1); rdT(2); rdT(3);
  stT(0); stT(1); stT(2); stT(3);
  STAMP(5);
}

extern "C" void kernel_launch(void* const* d_in, const int* in_sizes, int n_in,
                              void* d_out, int out_size, void* d_ws, size_t ws_size,
                              hipStream_t stream) {
  const __bf16* x = (const __bf16*)d_in[0];
  const __bf16* w = (const __bf16*)d_in[1];
  const __bf16* bias = (const __bf16*)d_in[2];
  const __bf16* gamma = (const __bf16*)d_in[3];
  const __bf16* beta = (const __bf16*)d_in[4];
  unsigned long long* gran = (unsigned long long*)d_ws;
  unsigned long long* stamps = (unsigned long long*)((char*)d_ws + (size_t)32 * 1024 * 1024);
  const unsigned ON = L * D * 2;
  const unsigned F = 0x00020000u, XN = (2 * L + KT - 2) * D * 2;
  conv_gn_gelu_kernel<<<256, 256, 0, stream>>>(w, KT * D * D * 2, F, x, XN, F, x + T * D, XN, F, d_out, ON, F, (__bf16*)d_out + L * D, ON, F,
                                               gran, GRAN_BYTES, F, bias, gamma, beta, stamps);
}
```
